```python
import torch

def _softmax(x, axis=-1):
    return torch.softmax(x, dim=axis)
def _gelu(x):
    return torch.nn.functional.gelu(x, approximate="tanh")
def _silu(x):
    return torch.nn.functional.silu(x)
def _sigmoid(x):
    return torch.sigmoid(x)
def _layernorm(x, axis=-1, eps=1e-5):
    m = x.mean(dim=axis, keepdim=True)
    v = x.var(dim=axis, keepdim=True, unbiased=False)
    return (x - m) / torch.sqrt(v + eps)
def _scatter_add(tgt, idx, src, axis=0):
    return tgt.clone().index_add_(axis, idx, src)
def _segment_sum(src, seg, n):
    out = torch.zeros((n,) + src.shape[1:], dtype=src.dtype)
    return out.index_add_(0, seg, src)
def _pad(x, pw):
    flat = tuple(v for pair in reversed(pw) for v in pair)
    return torch.nn.functional.pad(x, flat)

kernel_name = 'recurrent_scan__sliding_block_cached__glu_gated_swiglu__decode'

def setup_inputs():

    _w = (lambda a: (lambda t: t.to(torch.bfloat16) if t.is_floating_point() else t)(torch.as_tensor(a)))
    def _A_setup():
        B, Sq, D = 1, 1, 576
        Hq, Hkv, Sk, Dh = 32, 16, 65536, 128
        assert Hq % Hkv == 0, (Hq, Hkv)
        return {
            "x": _w((lambda *s: torch.randn(*s, dtype=torch.bfloat16) * 0.05)(B, Sq, D)),
            "k_cache": _w((lambda *s: torch.randn(*s, dtype=torch.bfloat16) * 3.2)(B, Hkv, Sk, Dh)),
            "v_cache": _w((lambda *s: torch.randn(*s, dtype=torch.bfloat16) * 3.2)(B, Hkv, Sk, Dh)),
            "wq": _w((lambda *s: torch.randn(*s, dtype=torch.bfloat16) * 3.2)(D, Hq * Dh) * (D ** -0.5)),
            "wo": _w((lambda *s: torch.randn(*s, dtype=torch.bfloat16) * 3.2)(Hq * Dh, D) * ((Hq * Dh) ** -0.5)),
        }
    _A_in = _A_setup()

    def _B_setup():
        B, S, D, F = 1, 1, 576, 4608
        return {
            "x": _w((lambda *s: torch.randn(*s, dtype=torch.bfloat16) * 0.05)(B, S, D)),
            "w1": _w((lambda *s: torch.randn(*s, dtype=torch.bfloat16) * 0.4)(D, F) * (2.0 / D) ** 0.5),
            "w3": _w((lambda *s: torch.randn(*s, dtype=torch.bfloat16) * 0.4)(D, F) * (2.0 / D) ** 0.5),
            "w2": _w((lambda *s: torch.randn(*s, dtype=torch.bfloat16) * 0.4)(F, D) * (2.0 / F) ** 0.5),
        }
    _B_in = _B_setup()

    return {
        "A_x": _A_in["x"],
        "A_k_cache": _A_in["k_cache"],
        "A_v_cache": _A_in["v_cache"],
        "A_wq": _A_in["wq"],
        "A_wo": _A_in["wo"],
        "B_x": _B_in["x"],
        "B_w1": _B_in["w1"],
        "B_w3": _B_in["w3"],
        "B_w2": _B_in["w2"],
    }

def reference(A_x, A_k_cache, A_v_cache, A_wq, A_wo, B_x, B_w1, B_w3, B_w2):
    def _A_ref(x, k_cache, v_cache, wq, wo):
        B, Sq, D = x.shape
        Hq, Dh = 32, 128
        Hkv, Sk = k_cache.shape[1], k_cache.shape[2]
        W = min(4096, max(256, Sk // 4))
        rep = Hq // Hkv
        q = (lambda x, *d: x.permute(*d))((x @ wq).reshape(B, Sq, Hq, Dh), 0, 2, 1, 3)
        k = (lambda x, n, axis: torch.repeat_interleave(x, n, dim=axis))(k_cache[:, :, Sk - W:, :], rep, axis=1)
        v = (lambda x, n, axis: torch.repeat_interleave(x, n, dim=axis))(v_cache[:, :, Sk - W:, :], rep, axis=1)
        scores = torch.einsum("bhqd,bhkd->bhqk", q, k) * (Dh ** -0.5)
        p = _softmax(scores, axis=-1)
        o = torch.einsum("bhqk,bhkd->bhqd", p, v)
        return (lambda x, *d: x.permute(*d))(o, 0, 2, 1, 3).reshape(B, Sq, Hq * Dh) @ wo

    def _B_ref(x, w1, w3, w2):
        gate = torch.einsum("bsd,df->bsf", x, w1)
        up = torch.einsum("bsd,df->bsf", x, w3)
        h = _silu(gate) * up
        out = torch.einsum("bsf,fd->bsd", h, w2)
        return out

    x = A_x
    xn = _layernorm(x, axis=-1)
    s = _A_ref(xn, A_k_cache, A_v_cache, A_wq, A_wo)
    g = _silu(_B_ref(xn, B_w1, B_w3, B_w2))
    return x + s * g
```

```cpp
#include <hip/hip_runtime.h>
#include <hip/hip_bf16.h>
#include <stdint.h>
#define MIMIC 1
typedef unsigned short u16;
typedef unsigned v4u __attribute__((ext_vector_type(4)));
constexpr int D = 576, HQ = 32, HKV = 16, SK = 65536, DH = 128, W = 4096, F = 4608, NQ = HQ * DH;
__device__ __forceinline__ float bf2f(u16 b) { return __builtin_bit_cast(float, (unsigned)b << 16); }
__device__ __forceinline__ float rbf(float f) {
  return (float)(__bf16)f;
}
__device__ __forceinline__ float mim(float f) { return MIMIC ? rbf(f) : f; }
__device__ __forceinline__ float lo(unsigned u) { return __builtin_bit_cast(float, u << 16); }
__device__ __forceinline__ float hi(unsigned u) { return __builtin_bit_cast(float, u & 0xffff0000u); }

__device__ void layernorm_to_lds(const u16* x, float* xn, float* red) {
  const int t = threadIdx.x;
  float s = 0.f;
  for (int i = t; i < D; i += blockDim.x) s += bf2f(x[i]);
  for (int o = 32; o; o >>= 1) s += __shfl_xor(s, o);
  if ((t & 63) == 0) red[t >> 6] = s;
  __syncthreads();
  float tot = 0.f;
  for (int w = 0; w < (int)(blockDim.x >> 6); ++w) tot += red[w];
  const float mean = tot / D;
  __syncthreads();
  float v = 0.f;
  for (int i = t; i < D; i += blockDim.x) { float d = bf2f(x[i]) - mean; v += d * d; }
  for (int o = 32; o; o >>= 1) v += __shfl_xor(v, o);
  if ((t & 63) == 0) red[t >> 6] = v;
  __syncthreads();
  float vt = 0.f;
  for (int w = 0; w < (int)(blockDim.x >> 6); ++w) vt += red[w];
  const float var = vt / D;
  const float m = mim(mean), vv = mim(var);
  const float den = mim(sqrtf(mim(vv + 1e-5f)));
  for (int i = t; i < D; i += blockDim.x) xn[i] = mim(mim(bf2f(x[i]) - m) / den);
  __syncthreads();
}

__global__ void __launch_bounds__(256) k1(const u16* x, const u16* wq, const u16* w1, const u16* w3, float* q, float* h) {
  __shared__ float xn[D]; __shared__ float red[4]; __shared__ float part[2][32][64];
  layernorm_to_lds(x, xn, red);
  const int t = threadIdx.x, c8 = t & 7, r = t >> 3;
  const bool isq = blockIdx.x < 64;
  const int cb = isq ? blockIdx.x * 64 : (blockIdx.x - 64) * 64;
  const int ld = isq ? NQ : F;
  const u16* a = isq ? wq : w1;
  float acc[8] = {0}, acc2[8] = {0};
  for (int k = r; k < D; k += 32) {
    v4u w = *(const v4u*)(a + (size_t)k * ld + cb + c8 * 8);
    float xv = xn[k];
    for (int j = 0; j < 4; ++j) { acc[2 * j] += xv * lo(w[j]); acc[2 * j + 1] += xv * hi(w[j]); }
    if (!isq) {
      v4u w_ = *(const v4u*)(w3 + (size_t)k * ld + cb + c8 * 8);
      for (int j = 0; j < 4; ++j) { acc2[2 * j] += xv * lo(w_[j]); acc2[2 * j + 1] += xv * hi(w_[j]); }
    }
  }
  for (int j = 0; j < 8; ++j) { part[0][r][c8 * 8 + j] = acc[j]; part[1][r][c8 * 8 + j] = acc2[j]; }
  __syncthreads();
  if (t < 64) {
    float s = 0.f, s2 = 0.f;
    for (int i = 0; i < 32; ++i) { s += part[0][i][t]; s2 += part[1][i][t]; }
    if (isq) q[cb + t] = mim(s);
    else { float g = mim(s), u = mim(s2); float sg = mim(g / (1.f + __expf(-g))); h[cb + t] = mim(sg * u); }
  }
}
__global__ void __launch_bounds__(256) k2(const u16* kc, const u16* vc, const float* q, float* po, float* pml) {
  __shared__ float sq[2][DH]; __shared__ float sc[2][256]; __shared__ float red[2][4]; __shared__ float oacc[16][2][DH];
  const int hk = blockIdx.x >> 4, sp = blockIdx.x & 15, t = threadIdx.x;
  sq[t >> 7][t & 127] = q[hk * 256 + t];
  __syncthreads();
  const size_t base = ((size_t)hk * SK + (SK - W) + sp * 256) * DH;
  {
    const u16* kr = kc + base + (size_t)t * DH;
    float s0 = 0.f, s1 = 0.f;
    for (int c = 0; c < 16; ++c) {
      v4u w = *(const v4u*)(kr + c * 8);
      for (int j = 0; j < 4; ++j) {
        s0 += sq[0][c * 8 + 2 * j] * lo(w[j]) + sq[0][c * 8 + 2 * j + 1] * hi(w[j]);
        s1 += sq[1][c * 8 + 2 * j] * lo(w[j]) + sq[1][c * 8 + 2 * j + 1] * hi(w[j]);
      }
    }
    const float scale = 0.08838834764831845f;
    s0 = mim(mim(s0) * scale); s1 = mim(mim(s1) * scale);
    float m0 = s0, m1 = s1;
    for (int o = 32; o; o >>= 1) { m0 = fmaxf(m0, __shfl_xor(m0, o)); m1 = fmaxf(m1, __shfl_xor(m1, o)); }
    if ((t & 63) == 0) { red[0][t >> 6] = m0; red[1][t >> 6] = m1; }
    __syncthreads();
    m0 = fmaxf(fmaxf(red[0][0], red[0][1]), fmaxf(red[0][2], red[0][3]));
    m1 = fmaxf(fmaxf(red[1][0], red[1][1]), fmaxf(red[1][2], red[1][3]));
    __syncthreads();
    float p0 = __expf(s0 - m0), p1 = __expf(s1 - m1);
    sc[0][t] = p0; sc[1][t] = p1;
    float l0 = p0, l1 = p1;
    for (int o = 32; o; o >>= 1) { l0 += __shfl_xor(l0, o); l1 += __shfl_xor(l1, o); }
    if ((t & 63) == 0) { red[0][t >> 6] = l0; red[1][t >> 6] = l1; }
    __syncthreads();
    if (t < 2) {
      float l = red[t][0] + red[t][1] + red[t][2] + red[t][3];
      pml[((hk * 2 + t) * 16 + sp) * 2] = t ? m1 : m0; pml[((hk * 2 + t) * 16 + sp) * 2 + 1] = l;
    }
  }
  const int c = t & 15, r = t >> 4;
  float a0[8] = {0}, a1[8] = {0};
  for (int k = r; k < 256; k += 16) {
    v4u w = *(const v4u*)(vc + base + (size_t)k * DH + c * 8);
    float p0 = sc[0][k], p1 = sc[1][k];
    for (int j = 0; j < 4; ++j) {
      a0[2 * j] += p0 * lo(w[j]); a0[2 * j + 1] += p0 * hi(w[j]);
      a1[2 * j] += p1 * lo(w[j]); a1[2 * j + 1] += p1 * hi(w[j]);
    }
  }
  for (int j = 0; j < 8; ++j) { oacc[r][0][c * 8 + j] = a0[j]; oacc[r][1][c * 8 + j] = a1[j]; }
  __syncthreads();
  float s = 0.f;
  for (int i = 0; i < 16; ++i) s += oacc[i][t >> 7][t & 127];
  po[((size_t)(hk * 2 + (t >> 7)) * 16 + sp) * DH + (t & 127)] = s;
}
__global__ void __launch_bounds__(288) k3(const float* po, const float* pml, const float* h, const u16* wo, const u16* w2, float* part) {
  __shared__ float vec[128]; __shared__ float red[4][D];
  const int b = blockIdx.x, t = threadIdx.x;
  const u16* w;
  if (b < 32) {
    if (t < 128) {
      float M = -INFINITY;
      for (int s = 0; s < 16; ++s) M = fmaxf(M, pml[(b * 16 + s) * 2]);
      float L = 0.f, o = 0.f;
      for (int s = 0; s < 16; ++s) { float e = __expf(pml[(b * 16 + s) * 2] - M); L += e * pml[(b * 16 + s) * 2 + 1]; o += e * po[((size_t)b * 16 + s) * DH + t]; }
      vec[t] = mim(o / L);
    }
    w = wo + (size_t)b * 128 * D;
  } else {
    if (t < 128) vec[t] = h[(b - 32) * 128 + t];
    w = w2 + (size_t)(b - 32) * 128 * D;
  }
  __syncthreads();
  const int c = t % 72, r = t / 72;
  float acc[8] = {0};
  for (int k = r; k < 128; k += 4) {
    v4u ww = *(const v4u*)(w + (size_t)k * D + c * 8);
    float xv = vec[k];
    for (int j = 0; j < 4; ++j) { acc[2 * j] += xv * lo(ww[j]); acc[2 * j + 1] += xv * hi(ww[j]); }
  }
  for (int j = 0; j < 8; ++j) red[r][c * 8 + j] = acc[j];
  __syncthreads();
  for (int i = t; i < D; i += 288) part[b * D + i] = red[0][i] + red[1][i] + red[2][i] + red[3][i];
}
__global__ void __launch_bounds__(576) k4(const float* part, const u16* x, __hip_bfloat16* out) {
  const int t = threadIdx.x;
  float s = 0.f, f = 0.f;
  for (int i = 0; i < 32; ++i) s += part[i * D + t];
  for (int i = 32; i < 68; ++i) f += part[i * D + t];
  s = mim(s); f = mim(f);
  float g = mim(f / (1.f + __expf(-f)));
  float r = bf2f(x[t]) + mim(s * g);
  out[t] = __float2bfloat16(r);
}
extern "C" void kernel_launch(void* const* d_in, const int* in_sizes, int n_in, void* d_out, int out_size, void* d_ws, size_t ws_size, hipStream_t stream) {
  const u16* x = (const u16*)d_in[0]; const u16* kc = (const u16*)d_in[1]; const u16* vc = (const u16*)d_in[2];
  const u16* wq = (const u16*)d_in[3]; const u16* wo = (const u16*)d_in[4];
  const u16* w1 = (const u16*)d_in[6]; const u16* w3 = (const u16*)d_in[7]; const u16* w2 = (const u16*)d_in[8];
  float* ws = (float*)d_ws;
  float* q = ws; float* h = ws + 4096; float* po = ws + 16384; float* pml = ws + 16384 + 65536; float* part = pml + 4096;
  k1<<<136, 256, 0, stream>>>(x, wq, w1, w3, q, h);
  k2<<<256, 256, 0, stream>>>(kc, vc, q, po, pml);
  k3<<<68, 288, 0, stream>>>(po, pml, h, wo, w2, part);
  k4<<<1, 576, 0, stream>>>(part, x, (__hip_bfloat16*)d_out);
}
```

```cpp
#include <hip/hip_runtime.h>

__global__ void __launch_bounds__(256, 1) fused_decode(void* x, void* k, void* v, void* wq, void* wo, void* w1, void* w3, void* w2, void* out, void* ws) {
  __shared__ __attribute__((aligned(16))) char lds[8192];
  auto ka = __builtin_amdgcn_kernarg_segment_ptr();
  unsigned bid = __builtin_amdgcn_workgroup_id_x(), tid = __builtin_amdgcn_workitem_id_x();
  asm volatile(
    "s_load_dwordx16 s[4:19], s[0:1], 0\n"
    "s_load_dwordx4 s[20:23], s[0:1], 64\n"
    "v_readfirstlane_b32 s3, v0\n"
    "s_and_b32 s28, s2, 7\n"
    "s_lshr_b32 s29, s2, 3\n"
    "v_and_b32 v0, 63, v0\n"
    "s_cmp_ge_u32 s29, 12\n"
    "s_cselect_b32 s30, 12, 0\n"
    "s_addc_u32 s31, s28, s28\n"
    "s_sub_u32 s30, s29, s30\n"
    "v_lshlrev_b32 v3, 4, v0\n"
    "s_lshr_b32 s3, s3, 6\n"
    "s_cbranch_scc1 .Lfd_stream\n"
    "s_getpc_b64 s[34:35]\n"
    ".Lfd_pc:\n"
    "s_and_b32 s36, s29, 7\n"
    "s_lshl_b32 s36, s36, 12\n"
    "v_and_b32 v1, 31, v0\n"
    "v_lshl_add_u32 v1, v1, 7, s36\n"
    "v_min_u32 v1, 0x4a54, v1\n"
    "global_load_dword v2, v1, s[34:35]\n"
    "v_lshlrev_b32 v4, 1, v0\n"
    "s_waitcnt lgkmcnt(0)\n"
    "s_memrealtime s[96:97]\n"
    "global_load_dwordx4 v[8:11], v3, s[4:5]\n"
    "global_load_ushort v12, v4, s[4:5] offset:1024\n"
    "s_branch .Lfd_comm\n"
    ".Lfd_stream:\n"
    "s_sub_u32 s3, s3, 1\n"
    "v_lshrrev_b32 v1, 3, v0\n"
    "v_and_b32 v2, 0x70, v3\n"
    "s_cmp_lt_u32 s29, 24\n"
    "s_cbranch_scc0 .Lfd_f\n"
    "s_lshr_b32 s37, s30, 1\n"
    "s_lshl1_add_u32 s44, s37, s37\n"
    "s_add_u32 s44, s44, s3\n"
    "s_and_b32 s36, s30, 1\n"
    "s_lshl1_add_u32 s38, s31, s36\n"
    "s_add_u32 s38, s38, 4\n"
    "s_and_b32 s38, s38, 31\n"
    "s_lshl_b32 s34, s44, 8\n"
    "s_add_u32 s34, s34, s38\n"
    "s_lshl_b32 s34, s34, 8\n"
    "v_lshl_add_u32 v4, v1, 13, v2\n"
    "v_add_u32 v4, s34, v4\n"
    "s_mov_b32 s35, 0x120000\n"
    "s_waitcnt lgkmcnt(0)\n"
    "s_memrealtime s[96:97]\n"
    "global_load_dwordx4 a[116:119], v4, s[10:11] nt\n"
    "global_load_dwordx4 a[120:123], v4, s[10:11] offset:128 nt\n"
    "v_add_u32 v4, s35, v4\n"
    "global_load_dwordx4 a[124:127], v4, s[10:11] nt\n"
    "global_load_dwordx4 a[128:131], v4, s[10:11] offset:128 nt\n"
    "v_add_u32 v4, s35, v4\n"
    "global_load_dwordx4 a[132:135], v4, s[10:11] nt\n"
    "global_load_dwordx4 a[136:139], v4, s[10:11] offset:128 nt\n"
    "v_add_u32 v4, s35, v4\n"
    "global_load_dwordx4 a[140:143], v4, s[10:11] nt\n"
    "global_load_dwordx4 a[144:147], v4, s[10:11] offset:128 nt\n"
    "s_branch .Lfd_a1\n"
    ".Lfd_f:\n"
    "s_sub_u32 s36, s29, 24\n"
    "s_add_u32 s38, s28, 1\n"
    "s_and_b32 s38, s38, 7\n"
    "s_mul_i32 s34, s36, 64\n"
    "s_add_u32 s34, s34, s38\n"
    "s_lshl1_add_u32 s34, s34, s34\n"
    "s_add_u32 s34, s34, s3\n"
    "s_mulk_i32 s34, 384\n"
    "v_mul_u32_u24 v4, 0x2400, v1\n"
    "v_add3_u32 v4, v4, v2, s34\n"
    "v_mov_b32 v6, v4\n"
    "s_mov_b32 s35, 589824\n"
    "s_waitcnt lgkmcnt(0)\n"
    "s_memrealtime s[96:97]\n"
    "global_load_dwordx4 a[0:3], v4, s[14:15] nt\n"
    "global_load_dwordx4 a[4:7], v4, s[14:15] offset:128 nt\n"
    "global_load_dwordx4 a[8:11], v4, s[14:15] offset:256 nt\n"
    "v_add_u32 v4, s35, v4\n"
    "global_load_dwordx4 a[12:15], v4, s[14:15] nt\n"
    "global_load_dwordx4 a[16:19], v4, s[14:15] offset:128 nt\n"
    "global_load_dwordx4 a[20:23], v4, s[14:15] offset:256 nt\n"
    "v_add_u32 v4, s35, v4\n"
    "global_load_dwordx4 a[24:27], v4, s[14:15] nt\n"
    "global_load_dwordx4 a[28:31], v4, s[14:15] offset:128 nt\n"
    "s_branch .Lfd_f1\n"
    ".Lfd_comm:\n"
    "s_load_dwordx4 s[24:27], s[22:23], 0\n"
    "s_waitcnt vmcnt(0)\n"
    "v_lshlrev_b32 v16, 16, v8\n"
    "v_and_b32 v17, 0xffff0000, v8\n"
    "v_lshlrev_b32 v18, 16, v9\n"
    "v_and_b32 v19, 0xffff0000, v9\n"
    "v_lshlrev_b32 v20, 16, v10\n"
    "v_and_b32 v21, 0xffff0000, v10\n"
    "v_lshlrev_b32 v22, 16, v11\n"
    "v_and_b32 v23, 0xffff0000, v11\n"
    "v_lshlrev_b32 v24, 16, v12\n"
    "v_pk_add_f32 v[8:9], v[16:17], v[18:19]\n"
    "v_pk_add_f32 v[10:11], v[20:21], v[22:23]\n"
    "v_pk_add_f32 v[8:9], v[8:9], v[10:11]\n"
    "v_add_f32 v8, v8, v9\n"
    "v_add_f32 v8, v8, v24\n"
    "s_nop 1\n"
    "v_add_f32_dpp v8, v8, v8 quad_perm:[1,0,3,2] row_mask:0xf bank_mask:0xf\n"
    "s_nop 1\n"
    "v_add_f32_dpp v8, v8, v8 quad_perm:[2,3,0,1] row_mask:0xf bank_mask:0xf\n"
    "s_nop 1\n"
    "v_add_f32_dpp v8, v8, v8 row_half_mirror row_mask:0xf bank_mask:0xf\n"
    "s_nop 1\n"
    "v_add_f32_dpp v8, v8, v8 row_ror:8 row_mask:0xf bank_mask:0xf\n"
    "v_mov_b32 v9, v8\n"
    "s_nop 1\n"
    "v_permlane16_swap_b32 v8, v9\n"
    "v_add_f32 v8, v8, v9\n"
    "v_mov_b32 v9, v8\n"
    "s_nop 1\n"
    "v_permlane32_swap_b32 v8, v9\n"
    "v_add_f32 v8, v8, v9\n"
    "v_mul_f32 v8, 0x3ae38e39, v8\n"
    "v_sub_f32 v9, v16, v8\n"
    "v_mul_f32 v10, v9, v9\n"
    "v_sub_f32 v9, v17, v8\n"
    "v_fmac_f32 v10, v9, v9\n"
    "v_sub_f32 v9, v18, v8\n"
    "v_fmac_f32 v10, v9, v9\n"
    "v_sub_f32 v9, v19, v8\n"
    "v_fmac_f32 v10, v9, v9\n"
    "v_sub_f32 v9, v20, v8\n"
    "v_fmac_f32 v10, v9, v9\n"
    "v_sub_f32 v9, v21, v8\n"
    "v_fmac_f32 v10, v9, v9\n"
    "v_sub_f32 v9, v22, v8\n"
    "v_fmac_f32 v10, v9, v9\n"
    "v_sub_f32 v9, v23, v8\n"
    "v_fmac_f32 v10, v9, v9\n"
    "v_sub_f32 v9, v24, v8\n"
    "v_fmac_f32 v10, v9, v9\n"
    "s_nop 1\n"
    "v_add_f32_dpp v10, v10, v10 quad_perm:[1,0,3,2] row_mask:0xf bank_mask:0xf\n"
    "s_nop 1\n"
    "v_add_f32_dpp v10, v10, v10 quad_perm:[2,3,0,1] row_mask:0xf bank_mask:0xf\n"
    "s_nop 1\n"
    "v_add_f32_dpp v10, v10, v10 row_half_mirror row_mask:0xf bank_mask:0xf\n"
    "s_nop 1\n"
    "v_add_f32_dpp v10, v10, v10 row_ror:8 row_mask:0xf bank_mask:0xf\n"
    "v_mov_b32 v9, v10\n"
    "s_nop 1\n"
    "v_permlane16_swap_b32 v10, v9\n"
    "v_add_f32 v10, v10, v9\n"
    "v_mov_b32 v9, v10\n"
    "s_nop 1\n"
    "v_permlane32_swap_b32 v10, v9\n"
    "v_add_f32 v10, v10, v9\n"
    "v_mul_f32 v10, 0x3ae38e39, v10\n"
    "v_cvt_pk_bf16_f32 v8, v8, v10\n"
    "v_and_b32 v10, 0xffff0000, v8\n"
    "v_lshlrev_b32 v8, 16, v8\n"
    "v_add_f32 v10, 0x3727c5ac, v10\n"
    "v_cvt_pk_bf16_f32 v10, v10, v10\n"
    "v_lshlrev_b32 v10, 16, v10\n"
    "v_sqrt_f32 v10, v10\n"
    "v_sub_f32 v16, v16, v8\n"
    "v_sub_f32 v17, v17, v8\n"
    "v_sub_f32 v18, v18, v8\n"
    "v_sub_f32 v19, v19, v8\n"
    "v_sub_f32 v20, v20, v8\n"
    "v_sub_f32 v21, v21, v8\n"
    "v_sub_f32 v22, v22, v8\n"
    "v_sub_f32 v23, v23, v8\n"
    "v_sub_f32 v24, v24, v8\n"
    "v_cvt_pk_bf16_f32 v10, v10, v10\n"
    "v_lshlrev_b32 v10, 16, v10\n"
    "v_rcp_f32 v11, v10\n"
    "v_cvt_pk_bf16_f32 v9, v16, v17\n"
    "v_lshlrev_b32 v16, 16, v9\n"
    "v_mul_f32 v12, v16, v11\n"
    "v_fma_f32 v16, -v12, v10, v16\n"
    "v_fma_f32 v16, v16, v11, v12\n"
    "v_and_b32 v17, 0xffff0000, v9\n"
    "v_mul_f32 v12, v17, v11\n"
    "v_fma_f32 v17, -v12, v10, v17\n"
    "v_fma_f32 v17, v17, v11, v12\n"
    "v_cvt_pk_bf16_f32 v28, v16, v17\n"
    "v_cvt_pk_bf16_f32 v9, v18, v19\n"
    "v_lshlrev_b32 v18, 16, v9\n"
    "v_mul_f32 v12, v18, v11\n"
    "v_fma_f32 v18, -v12, v10, v18\n"
    "v_fma_f32 v18, v18, v11, v12\n"
    "v_and_b32 v19, 0xffff0000, v9\n"
    "v_mul_f32 v12, v19, v11\n"
    "v_fma_f32 v19, -v12, v10, v19\n"
    "v_fma_f32 v19, v19, v11, v12\n"
    "v_cvt_pk_bf16_f32 v29, v18, v19\n"
    "v_cvt_pk_bf16_f32 v9, v20, v21\n"
    "v_lshlrev_b32 v20, 16, v9\n"
    "v_mul_f32 v12, v20, v11\n"
    "v_fma_f32 v20, -v12, v10, v20\n"
    "v_fma_f32 v20, v20, v11, v12\n"
    "v_and_b32 v21, 0xffff0000, v9\n"
    "v_mul_f32 v12, v21, v11\n"
    "v_fma_f32 v21, -v12, v10, v21\n"
    "v_fma_f32 v21, v21, v11, v12\n"
    "v_cvt_pk_bf16_f32 v30, v20, v21\n"
    "v_cvt_pk_bf16_f32 v9, v22, v23\n"
    "v_lshlrev_b32 v22, 16, v9\n"
    "v_mul_f32 v12, v22, v11\n"
    "v_fma_f32 v22, -v12, v10, v22\n"
    "v_fma_f32 v22, v22, v11, v12\n"
    "v_and_b32 v23, 0xffff0000, v9\n"
    "v_mul_f32 v12, v23, v11\n"
    "v_fma_f32 v23, -v12, v10, v23\n"
    "v_fma_f32 v23, v23, v11, v12\n"
    "v_cvt_pk_bf16_f32 v31, v22, v23\n"
    "v_cvt_pk_bf16_f32 v9, v24, v24\n"
    "v_lshlrev_b32 v24, 16, v9\n"
    "v_mul_f32 v12, v24, v11\n"
    "v_fma_f32 v24, -v12, v10, v24\n"
    "v_fma_f32 v24, v24, v11, v12\n"
    "v_cvt_pk_bf16_f32 v32, v24, v24\n"
    "ds_write_b128 v3, v[28:31] offset:0\n"
    "ds_write_b16 v4, v32 offset:1024\n"
    "v_lshrrev_b32 v14, 5, v0\n"
    "s_waitcnt lgkmcnt(0)\n"
    "s_barrier\n"
    "s_add_u32 s24, s24, 1\n"
    "s_add_u32 s25, s25, 1\n"
    "s_add_u32 s26, s26, 1\n"
    "s_add_u32 s27, s27, 1\n"
    "s_movk_i32 s45, 0x3ff\n"
    "s_and_b32 s40, s24, s45\n"
    "s_and_b32 s41, s25, s45\n"
    "s_and_b32 s42, s26, s45\n"
    "s_and_b32 s43, s27, s45\n"
    "s_cmp_lt_u32 s29, 24\n"
    "s_cbranch_scc0 .Lfd_fc\n"
    "v_bfe_u32 v8, v0, 1, 3\n"
    "v_and_b32 v9, 1, v0\n"
    "v_lshl_add_u32 v8, v9, 4, v8\n"
    "v_bfe_u32 v9, v0, 4, 1\n"
    "v_lshl_add_u32 v8, v9, 6, v8\n"
    "v_lshlrev_b32 v15, 4, v8\n"
    "s_and_b32 s16, s40, 63\n"
    "s_and_b32 s17, s41, 63\n"
    "s_and_b32 s18, s42, 63\n"
    "s_and_b32 s19, s43, 63\n"
    "s_mul_i32 s36, s31, 6144\n"
    "s_add_u32 s36, s36, 4096\n"
    "s_add_u32 s63, s31, 2\n"
    "s_and_b32 s63, s63, 15\n"
    "s_mul_i32 s37, s63, 12\n"
    "s_add_u32 s37, s37, s30\n"
    "s_lshl_b32 s37, s37, 9\n"
    "s_add_u32 s37, s37, 4096\n"
    "v_add_u32 v8, s37, v3\n"
    "s_add_u32 s36, s36, 2048\n"
    "v_add_u32 v9, s36, v3\n"
    "s_barrier\n"
    "ds_read_b128 v[16:19], v15 offset:2048\n"
    "ds_read_b128 v[20:23], v15 offset:2688\n"
    "ds_read_b128 v[24:27], v15 offset:4096\n"
    "ds_read_b128 v[28:31], v15 offset:4736\n"
    "ds_read_b128 v[32:35], v15 offset:6144\n"
    "ds_read_b128 v[36:39], v15 offset:6784\n"
    "s_waitcnt lgkmcnt(0)\n"
    "v_pk_add_f32 v[16:17], v[16:17], v[20:21]\n"
    "v_pk_add_f32 v[18:19], v[18:19], v[22:23]\n"
    "v_pk_add_f32 v[24:25], v[24:25], v[28:29]\n"
    "v_pk_add_f32 v[26:27], v[26:27], v[30:31]\n"
    "v_pk_add_f32 v[32:33], v[32:33], v[36:37]\n"
    "v_pk_add_f32 v[34:35], v[34:35], v[38:39]\n"
    "v_pk_add_f32 v[16:17], v[16:17], v[24:25]\n"
    "v_pk_add_f32 v[18:19], v[18:19], v[26:27]\n"
    "v_pk_add_f32 v[16:17], v[16:17], v[32:33]\n"
    "v_pk_add_f32 v[18:19], v[18:19], v[34:35]\n"
    "v_add_u32 v16, 32, v16\n"
    "v_add_u32 v17, 32, v17\n"
    "v_add_u32 v18, 32, v18\n"
    "v_add_u32 v19, 32, v19\n"
    "v_bfi_b32 v16, 63, s16, v16\n"
    "v_bfi_b32 v17, 63, s17, v17\n"
    "v_bfi_b32 v18, 63, s18, v18\n"
    "v_bfi_b32 v19, 63, s19, v19\n"
    "s_mov_b32 exec_hi, 0\n"
    "global_store_dwordx4 v8, v[16:19], s[22:23] sc1\n"
    "s_mov_b64 exec, -1\n"
    "s_branch .Lfd_ac2\n"
    ".Lfd_f1:\n"
    "s_load_dwordx4 s[24:27], s[22:23], 0\n"
    "s_lshl3_add_u32 s41, s28, s36\n"
    "s_mul_i32 s34, s41, 216\n"
    "s_add_u32 s34, s34, s3\n"
    "s_mulk_i32 s34, 384\n"
    "v_mul_u32_u24 v5, 0x480, v1\n"
    "v_add3_u32 v5, v5, v2, s34\n"
    "s_movk_i32 s37, 0x2400\n"
    "s_lshr_b32 s34, 0x400000, s3\n"
    "s_sub_u32 s34, 0x600000, s34\n"
    "s_load_dword s40, s[14:15], s34\n"
    "v_and_b32 v42, 1, v0\n"
    "v_cmp_eq_u32 vcc, 1, v42\n"
    "v_mov_b32 v42, 0x0c0c0100\n"
    "v_mov_b32 v43, 0x01000c0c\n"
    "v_cndmask_b32 v42, v42, v43, vcc\n"
    "v_mov_b32 v43, 0x0c0c0302\n"
    "v_mov_b32 v44, 0x03020c0c\n"
    "v_cndmask_b32 v43, v43, v44, vcc\n"
    "v_bfe_u32 v44, v0, 1, 2\n"
    "v_mov_b32 v45, 0x0c0c0c0c\n"
    "v_cmp_eq_u32 vcc, 0, v44\n"
    "s_nop 1\n"
    "v_cndmask_b32 v10, v45, v42, vcc\n"
    "v_cndmask_b32 v14, v45, v43, vcc\n"
    "v_cmp_eq_u32 vcc, 1, v44\n"
    "s_nop 1\n"
    "v_cndmask_b32 v11, v45, v42, vcc\n"
    "v_cndmask_b32 v15, v45, v43, vcc\n"
    "v_cmp_eq_u32 vcc, 2, v44\n"
    "s_nop 1\n"
    "v_cndmask_b32 v12, v45, v42, vcc\n"
    "v_cndmask_b32 v16, v45, v43, vcc\n"
    "v_cmp_eq_u32 vcc, 3, v44\n"
    "s_nop 1\n"
    "v_cndmask_b32 v13, v45, v42, vcc\n"
    "v_cndmask_b32 v17, v45, v43, vcc\n"
    "s_mul_i32 s34, s36, 16\n"
    "v_lshlrev_b32 v49, 1, v1\n"
    "v_add_u32 v42, s34, v49\n"
    "global_load_dwordx4 a[32:35], v4, s[14:15] offset:256 nt\n"
    "v_add_u32 v4, s35, v4\n"
    "s_waitcnt vmcnt(8)\n"
    "global_load_dwordx4 a[36:39], v4, s[14:15] nt\n"
    "s_barrier\n"
    "ds_read_u16 v52, v42 offset:0\n"
    "ds_read_u16 v53, v42 offset:128\n"
    "ds_read_u16 v54, v42 offset:256\n"
    "ds_read_u16 v55, v42 offset:384\n"
    "ds_read_u16 v56, v42 offset:512\n"
    "ds_read_u16 v57, v42 offset:640\n"
    "ds_read_u16 v58, v42 offset:768\n"
    "ds_read_u16 v59, v42 offset:896\n"
    "ds_read_u16 v60, v42 offset:1024\n"
    "s_waitcnt lgkmcnt(0)\n"
    "s_add_u32 s24, s24, 1\n"
    "s_add_u32 s25, s25, 1\n"
    "s_add_u32 s26, s26, 1\n"
    "s_add_u32 s27, s27, 1\n"
    "s_waitcnt vmcnt(8)\n"
    "global_load_dwordx4 a[40:43], v4, s[14:15] offset:128 nt\n"
    "v_and_b32 v42, 1, v0\n"
    "v_cmp_eq_u32 vcc, 1, v42\n"
    "v_mov_b32 v95, s24\n"
    "v_mov_b32 v42, s26\n"
    "v_cndmask_b32 v95, v95, v42, vcc\n"
    "v_mov_b32 v96, s25\n"
    "v_mov_b32 v42, s27\n"
    "v_cndmask_b32 v96, v96, v42, vcc\n"
    "v_mov_b32 v97, 0x3ff\n"
    "s_lshl3_add_u32 s34, s38, s36\n"
    "s_mul_i32 s34, s34, 3\n"
    "s_add_u32 s34, s34, s3\n"
    "s_mulk_i32 s34, 1536\n"
    "s_add_u32 s34, s34, 102400\n"
    "v_lshrrev_b32 v42, 4, v0\n"
    "v_lshl_add_u32 v42, v42, 7, s34\n"
    "v_add_u32 v67, v42, v2\n"
    "s_waitcnt vmcnt(8)\n"
    "global_load_dwordx4 a[44:47], v4, s[14:15] offset:256 nt\n"
    "v_add_u32 v4, s35, v4\n"
    "s_lshl1_add_u32 s40, s3, s3\n"
    "s_add_u32 s41, s41, 64\n"
    "s_lshl_b32 s34, s40, 15\n"
    "s_lshl_b32 s45, s41, 7\n"
    "s_add_u32 s34, s34, s45\n"
    "s_add_u32 s34, s34, 618496\n"
    "v_lshrrev_b32 v42, 5, v0\n"
    "v_lshl_add_u32 v64, v42, 14, s34\n"
    "v_bfe_u32 v42, v0, 4, 1\n"
    "v_lshl_add_u32 v64, v42, 6, v64\n"
    "v_and_b32 v42, 7, v0\n"
    "v_lshl_add_u32 v64, v42, 3, v64\n"
    "s_mov_b32 s46, 0x00ff00ff\n"
    "s_mov_b32 s47, 0x00ff00ff\n"
    "v_add_u32 v65, 0x8000, v64\n"
    "v_add_u32 v66, 0x8000, v65\n"
    "s_waitcnt vmcnt(8)\n"
    "global_load_dwordx4 a[48:51], v4, s[14:15] nt\n"
    "s_add_u32 s40, s3, 1\n"
    "s_lshl3_add_u32 s39, s36, s28\n"
    "s_min_u32 s58, s39, 35\n"
    "s_lshr_b32 s59, s58, 1\n"
    "s_lshl2_add_u32 s59, s59, s40\n"
    "s_lshl_b32 s59, s59, 12\n"
    "s_and_b32 s60, s58, 1\n"
    "s_lshl_b32 s60, s60, 6\n"
    "s_add_u32 s59, s59, s60\n"
    "s_add_u32 s59, s59, 618496\n"
    "v_lshrrev_b32 v42, 3, v0\n"
    "v_lshl_add_u32 v170, v42, 7, s59\n"
    "v_and_b32 v42, 7, v0\n"
    "v_lshl_add_u32 v170, v42, 3, v170\n"
    "v_lshrrev_b32 v171, 5, v0\n"
    "v_lshl_add_u32 v171, v171, 3, v42\n"
    "s_lshl_b32 s59, s40, 2\n"
    "v_lshl_add_u32 v171, v171, 4, s59\n"
    "s_movk_i32 s45, 0x3ff\n"
    "v_and_b32 v172, s45, v95\n"
    "v_and_b32 v173, s45, v96\n"
    "v_mov_b64 v[174:175], 0\n"
    "s_mov_b32 s61, s40\n"
    "v_perm_b32 v22, v52, v52, v10\n"
    "v_perm_b32 v23, v52, v52, v11\n"
    "v_perm_b32 v24, v52, v52, v12\n"
    "v_perm_b32 v25, v52, v52, v13\n"
    "s_nop 1\n"
    "v_mfma_f32_16x16x32_bf16 v[68:71], v[22:25], a[0:3], 0\n"
    "v_mfma_f32_16x16x32_bf16 v[72:75], v[22:25], a[4:7], 0\n"
    "v_mfma_f32_16x16x32_bf16 v[76:79], v[22:25], a[8:11], 0\n"
    "v_perm_b32 v26, v53, v53, v10\n"
    "v_perm_b32 v27, v53, v53, v11\n"
    "v_perm_b32 v28, v53, v53, v12\n"
    "v_perm_b32 v29, v53, v53, v13\n"
    "s_nop 1\n"
    "v_mfma_f32_16x16x32_bf16 v[68:71], v[26:29], a[12:15], v[68:71]\n"
    "s_waitcnt vmcnt(8)\n"
    "global_load_dwordx4 a[52:55], v4, s[14:15] offset:128 nt\n"
    "v_mfma_f32_16x16x32_bf16 v[72:75], v[26:29], a[16:19], v[72:75]\n"
    "s_waitcnt vmcnt(8)\n"
    "global_load_dwordx4 a[56:59], v4, s[14:15] offset:256 nt\n"
    "v_add_u32 v4, s35, v4\n"
    "v_mfma_f32_16x16x32_bf16 v[76:79], v[26:29], a[20:23], v[76:79]\n"
    "s_branch .Lfd_f2\n"
    ".Lfd_a1:\n"
    "s_load_dwordx4 s[24:27], s[22:23], 0\n"
    "s_mov_b32 s43, 0\n"
    "s_lshl1_add_u32 s50, s30, s30\n"
    "s_add_u32 s50, s50, s3\n"
    "s_lshl_b32 s34, s31, 24\n"
    "s_add_u32 s34, s34, 15728640\n"
    "s_add_u32 s6, s6, s34\n"
    "s_addc_u32 s7, s7, 0\n"
    "s_add_u32 s8, s8, s34\n"
    "s_addc_u32 s9, s9, 0\n"
    "s_mul_i32 s40, s50, 114\n"
    "s_lshr_b32 s41, s40, 6\n"
    "s_add_u32 s40, s40, 114\n"
    "s_lshr_b32 s40, s40, 6\n"
    "s_sub_u32 s40, s40, s41\n"
    "s_bitcmp1_b32 s40, 0\n"
    "s_cselect_b32 s42, 0xffff, -1\n"
    "s_add_u32 s41, s41, 4032\n"
    "s_lshl_b32 s41, s41, 8\n"
    "v_add_u32 v7, s41, v3\n"
    "s_mul_i32 s50, s50, 1024\n"
    "v_add_u32 v5, s50, v3\n"
    "v_add_u32 v6, s50, v3\n"
    "s_mov_b32 s50, 36864\n"
    "v_accvgpr_write_b32 a112, 0\n"
    "v_accvgpr_write_b32 a113, 0\n"
    "v_accvgpr_write_b32 a114, 0\n"
    "v_accvgpr_write_b32 a115, 0\n"
    "v_and_b32 v42, 1, v0\n"
    "v_cmp_eq_u32 vcc, 1, v42\n"
    "v_mov_b32 v42, 0x0c0c0100\n"
    "v_mov_b32 v43, 0x01000c0c\n"
    "v_cndmask_b32 v42, v42, v43, vcc\n"
    "v_mov_b32 v43, 0x0c0c0302\n"
    "v_mov_b32 v44, 0x03020c0c\n"
    "v_cndmask_b32 v43, v43, v44, vcc\n"
    "v_bfe_u32 v44, v0, 1, 2\n"
    "v_mov_b32 v45, 0x0c0c0c0c\n"
    "v_cmp_eq_u32 vcc, 0, v44\n"
    "s_nop 1\n"
    "v_cndmask_b32 v10, v45, v42, vcc\n"
    "v_cndmask_b32 v14, v45, v43, vcc\n"
    "v_cmp_eq_u32 vcc, 1, v44\n"
    "s_nop 1\n"
    "v_cndmask_b32 v11, v45, v42, vcc\n"
    "v_cndmask_b32 v15, v45, v43, vcc\n"
    "v_cmp_eq_u32 vcc, 2, v44\n"
    "s_nop 1\n"
    "v_cndmask_b32 v12, v45, v42, vcc\n"
    "v_cndmask_b32 v16, v45, v43, vcc\n"
    "v_cmp_eq_u32 vcc, 3, v44\n"
    "s_nop 1\n"
    "v_cndmask_b32 v13, v45, v42, vcc\n"
    "v_cndmask_b32 v17, v45, v43, vcc\n"
    "s_and_b32 s36, s30, 3\n"
    "s_lshr_b32 s37, s30, 2\n"
    "s_lshl2_add_u32 s38, s31, s36\n"
    "s_add_u32 s38, s38, 8\n"
    "s_and_b32 s38, s38, 63\n"
    "s_lshl1_add_u32 s39, s37, s37\n"
    "s_add_u32 s39, s39, s3\n"
    "s_lshl_b32 s34, s44, 4\n"
    "v_lshl_add_u32 v42, v1, 1, s34\n"
    "s_lshl_b32 s34, s3, 11\n"
    "v_add_u32 v43, s34, v3\n"
    "global_load_dwordx4 v[100:103], v5, s[6:7] offset:0 nt\n"
    "v_add_u32 v5, s50, v5\n"
    "s_waitcnt vmcnt(8)\n"
    "global_load_dwordx4 v[104:107], v5, s[6:7] offset:0 nt\n"
    "v_add_u32 v5, s50, v5\n"
    "s_barrier\n"
    "ds_read_u16 v50, v42 offset:0\n"
    "ds_read_u16 v51, v42 offset:288\n"
    "ds_read_u16 v52, v42 offset:576\n"
    "ds_read_u16 v53, v42 offset:864\n"
    "s_waitcnt lgkmcnt(0)\n"
    "s_add_u32 s24, s24, 1\n"
    "s_add_u32 s25, s25, 1\n"
    "s_add_u32 s26, s26, 1\n"
    "s_add_u32 s27, s27, 1\n"
    "v_perm_b32 v22, v50, v50, v10\n"
    "v_perm_b32 v23, v50, v50, v11\n"
    "v_perm_b32 v24, v50, v50, v12\n"
    "v_perm_b32 v25, v50, v50, v13\n"
    "s_nop 1\n"
    "v_mfma_f32_16x16x32_bf16 v[30:33], v[22:25], a[116:119], 0\n"
    "s_waitcnt vmcnt(8)\n"
    "global_load_dwordx4 v[108:111], v5, s[6:7] offset:0 nt\n"
    "v_add_u32 v5, s50, v5\n"
    "v_mfma_f32_16x16x32_bf16 v[34:37], v[22:25], a[120:123], 0\n"
    "v_perm_b32 v26, v51, v51, v10\n"
    "v_perm_b32 v27, v51, v51, v11\n"
    "v_perm_b32 v28, v51, v51, v12\n"
    "v_perm_b32 v29, v51, v51, v13\n"
    "s_waitcnt vmcnt(8)\n"
    "global_load_dwordx4 v[112:115], v5, s[6:7] offset:0 nt\n"
    "v_add_u32 v5, s50, v5\n"
    "v_mfma_f32_16x16x32_bf16 v[30:33], v[26:29], a[124:127], v[30:33]\n"
    "s_waitcnt vmcnt(8)\n"
    "global_load_dwordx4 v[116:119], v5, s[6:7] offset:0 nt\n"
    "v_add_u32 v5, s50, v5\n"
    "v_mfma_f32_16x16x32_bf16 v[34:37], v[26:29], a[128:131], v[34:37]\n"
    "v_perm_b32 v22, v52, v52, v10\n"
    "v_perm_b32 v23, v52, v52, v11\n"
    "v_perm_b32 v24, v52, v52, v12\n"
    "v_perm_b32 v25, v52, v52, v13\n"
    "s_waitcnt vmcnt(8)\n"
    "global_load_dwordx4 v[120:123], v5, s[6:7] offset:0 nt\n"
    "v_add_u32 v5, s50, v5\n"
    "v_mfma_f32_16x16x32_bf16 v[30:33], v[22:25], a[132:135], v[30:33]\n"
    "s_waitcnt vmcnt(8)\n"
    "global_load_dwordx4 v[124:127], v5, s[6:7] offset:0 nt\n"
    "v_add_u32 v5, s50, v5\n"
    "v_mfma_f32_16x16x32_bf16 v[34:37], v[22:25], a[136:139], v[34:37]\n"
    "v_perm_b32 v26, v53, v53, v10\n"
    "v_perm_b32 v27, v53, v53, v11\n"
    "v_perm_b32 v28, v53, v53, v12\n"
    "v_perm_b32 v29, v53, v53, v13\n"
    "s_waitcnt vmcnt(8)\n"
    "global_load_dwordx4 v[128:131], v5, s[6:7] offset:0 nt\n"
    "v_add_u32 v5, s50, v5\n"
    "v_mfma_f32_16x16x32_bf16 v[30:33], v[26:29], a[140:143], v[30:33]\n"
    "s_waitcnt vmcnt(8)\n"
    "s_mov_b64 exec, s[42:43]\n"
    "global_load_dwordx4 v[212:215], v7, s[6:7] nt\n"
    "s_mov_b64 exec, -1\n"
    "v_mfma_f32_16x16x32_bf16 v[34:37], v[26:29], a[144:147], v[34:37]\n"
    "s_mul_i32 s34, s38, 576\n"
    "s_add_u32 s34, s34, s39\n"
    "s_lshl_b32 s34, s34, 7\n"
    "s_add_u32 s34, s34, 4032\n"
    "v_mul_u32_u24 v9, 0x2400, v1\n"
    "v_add3_u32 v9, v9, v2, s34\n"
    "ds_write_b128 v43, v[30:33] offset:2048\n"
    "s_nop 0\n"
    "ds_write_b128 v43, v[34:37] offset:3072\n"
    "s_waitcnt lgkmcnt(0)\n"
    "s_barrier\n"
    "s_waitcnt vmcnt(8)\n"
    "global_load_dwordx4 v[132:135], v5, s[6:7] offset:0 nt\n"
    "v_add_u32 v5, s50, v5\n"
    "s_branch .Lfd_a2\n"
    ".Lfd_f2:\n"
    "v_perm_b32 v22, v54, v54, v10\n"
    "v_perm_b32 v23, v54, v54, v11\n"
    "v_perm_b32 v24, v54, v54, v12\n"
    "v_perm_b32 v25, v54, v54, v13\n"
    "s_waitcnt vmcnt(8)\n"
    "global_load_dwordx4 a[60:63], v4, s[14:15] nt\n"
    "v_mfma_f32_16x16x32_bf16 v[68:71], v[22:25], a[24:27], v[68:71]\n"
    "s_waitcnt vmcnt(8)\n"
    "global_load_dwordx4 a[64:67], v4, s[14:15] offset:128 nt\n"
    "v_mfma_f32_16x16x32_bf16 v[72:75], v[22:25], a[28:31], v[72:75]\n"
    "s_waitcnt vmcnt(8)\n"
    "global_load_dwordx4 a[68:71], v4, s[14:15] offset:256 nt\n"
    "v_add_u32 v4, s35, v4\n"
    "v_mfma_f32_16x16x32_bf16 v[76:79], v[22:25], a[32:35], v[76:79]\n"
    "v_perm_b32 v26, v55, v55, v10\n"
    "v_perm_b32 v27, v55, v55, v11\n"
    "v_perm_b32 v28, v55, v55, v12\n"
    "v_perm_b32 v29, v55, v55, v13\n"
    "s_memrealtime s[98:99]\n"
    "s_waitcnt vmcnt(8)\n"
    "s_waitcnt lgkmcnt(0)\n"
    "s_sub_u32 s98, s98, s96\n"
    "s_cmpk_lt_u32 s98, 164\n"
    "s_cselect_b64 vcc, -1, 0\n"
    "s_cbranch_vccz .Lfd_qf18\n"
    "s_waitcnt vmcnt(5)\n"
    ".Lfd_qf18:\n"
    "global_load_dwordx4 a[72:75], v4, s[14:15] nt\n"
    "v_mfma_f32_16x16x32_bf16 v[68:71], v[26:29], a[36:39], v[68:71]\n"
    "s_waitcnt vmcnt(8)\n"
    "s_cbranch_vccz .Lfd_qf19\n"
    "s_waitcnt vmcnt(5)\n"
    ".Lfd_qf19:\n"
    "global_load_dwordx4 a[76:79], v4, s[14:15] offset:128 nt\n"
    "v_mfma_f32_16x16x32_bf16 v[72:75], v[26:29], a[40:43], v[72:75]\n"
    "s_waitcnt vmcnt(8)\n"
    "s_cbranch_vccz .Lfd_qf20\n"
    "s_waitcnt vmcnt(5)\n"
    ".Lfd_qf20:\n"
    "global_load_dwordx4 a[80:83], v4, s[14:15] offset:256 nt\n"
    "v_add_u32 v4, s35, v4\n"
    "v_mfma_f32_16x16x32_bf16 v[76:79], v[26:29], a[44:47], v[76:79]\n"
    "v_perm_b32 v22, v56, v56, v10\n"
    "v_perm_b32 v23, v56, v56, v11\n"
    "v_perm_b32 v24, v56, v56, v12\n"
    "v_perm_b32 v25, v56, v56, v13\n"
    "s_waitcnt vmcnt(8)\n"
    "s_cbranch_vccz .Lfd_qf21\n"
    "s_waitcnt vmcnt(5)\n"
    ".Lfd_qf21:\n"
    "global_load_dwordx4 a[84:87], v4, s[14:15] nt\n"
    "v_mfma_f32_16x16x32_bf16 v[68:71], v[22:25], a[48:51], v[68:71]\n"
    "s_waitcnt vmcnt(8)\n"
    "s_cbranch_vccz .Lfd_qf22\n"
    "s_waitcnt vmcnt(5)\n"
    ".Lfd_qf22:\n"
    "global_load_dwordx4 a[88:91], v4, s[14:15] offset:128 nt\n"
    "v_mfma_f32_16x16x32_bf16 v[72:75], v[22:25], a[52:55], v[72:75]\n"
    "s_memrealtime s[98:99]\n"
    "s_waitcnt vmcnt(8)\n"
    "s_waitcnt lgkmcnt(0)\n"
    "s_sub_u32 s98, s98, s96\n"
    "s_cmpk_lt_u32 s98, 218\n"
    "s_cselect_b64 vcc, -1, 0\n"
    "s_cbranch_vccz .Lfd_qf23\n"
    "s_waitcnt vmcnt(5)\n"
    ".Lfd_qf23:\n"
    "global_load_dwordx4 a[92:95], v4, s[14:15] offset:256 nt\n"
    "v_add_u32 v4, s35, v4\n"
    "v_mfma_f32_16x16x32_bf16 v[76:79], v[22:25], a[56:59], v[76:79]\n"
    "v_perm_b32 v26, v57, v57, v10\n"
    "v_perm_b32 v27, v57, v57, v11\n"
    "v_perm_b32 v28, v57, v57, v12\n"
    "v_perm_b32 v29, v57, v57, v13\n"
    "s_waitcnt vmcnt(8)\n"
    "s_cbranch_vccz .Lfd_qf24\n"
    "s_waitcnt vmcnt(5)\n"
    ".Lfd_qf24:\n"
    "global_load_dwordx4 a[96:99], v4, s[14:15] nt\n"
    "v_mfma_f32_16x16x32_bf16 v[68:71], v[26:29], a[60:63], v[68:71]\n"
    "s_waitcnt vmcnt(8)\n"
    "s_cbranch_vccz .Lfd_qf25\n"
    "s_waitcnt vmcnt(5)\n"
    ".Lfd_qf25:\n"
    "global_load_dwordx4 a[100:103], v4, s[14:15] offset:128 nt\n"
    "v_mfma_f32_16x16x32_bf16 v[72:75], v[26:29], a[64:67], v[72:75]\n"
    "s_waitcnt vmcnt(8)\n"
    "s_cbranch_vccz .Lfd_qf26\n"
    "s_waitcnt vmcnt(5)\n"
    ".Lfd_qf26:\n"
    "global_load_dwordx4 a[104:107], v4, s[14:15] offset:256 nt\n"
    "v_mfma_f32_16x16x32_bf16 v[76:79], v[26:29], a[68:71], v[76:79]\n"
    "v_perm_b32 v22, v58, v58, v10\n"
    "v_perm_b32 v23, v58, v58, v11\n"
    "v_perm_b32 v24, v58, v58, v12\n"
    "v_perm_b32 v25, v58, v58, v13\n"
    "s_waitcnt vmcnt(8)\n"
    "s_cbranch_vccz .Lfd_qf27\n"
    "s_waitcnt vmcnt(5)\n"
    ".Lfd_qf27:\n"
    "global_load_dwordx4 a[108:111], v6, s[16:17] nt\n"
    "v_mfma_f32_16x16x32_bf16 v[68:71], v[22:25], a[72:75], v[68:71]\n"
    "s_memrealtime s[98:99]\n"
    "s_waitcnt vmcnt(8)\n"
    "s_waitcnt lgkmcnt(0)\n"
    "s_sub_u32 s98, s98, s96\n"
    "s_cmpk_lt_u32 s98, 272\n"
    "s_cselect_b64 vcc, -1, 0\n"
    "s_cbranch_vccz .Lfd_qf28\n"
    "s_waitcnt vmcnt(5)\n"
    ".Lfd_qf28:\n"
    "global_load_dwordx4 a[112:115], v6, s[16:17] offset:128 nt\n"
    "v_mfma_f32_16x16x32_bf16 v[72:75], v[22:25], a[76:79], v[72:75]\n"
    "s_waitcnt vmcnt(8)\n"
    "s_cbranch_vccz .Lfd_qf29\n"
    "s_waitcnt vmcnt(5)\n"
    ".Lfd_qf29:\n"
    "global_load_dwordx4 a[116:119], v6, s[16:17] offset:256 nt\n"
    "v_add_u32 v6, s35, v6\n"
    "v_mfma_f32_16x16x32_bf16 v[76:79], v[22:25], a[80:83], v[76:79]\n"
    "v_perm_b32 v26, v59, v59, v10\n"
    "v_perm_b32 v27, v59, v59, v11\n"
    "v_perm_b32 v28, v59, v59, v12\n"
    "v_perm_b32 v29, v59, v59, v13\n"
    "s_waitcnt vmcnt(8)\n"
    "s_cbranch_vccz .Lfd_qf30\n"
    "s_waitcnt vmcnt(5)\n"
    ".Lfd_qf30:\n"
    "global_load_dwordx4 a[120:123], v6, s[16:17] nt\n"
    "v_mfma_f32_16x16x32_bf16 v[68:71], v[26:29], a[84:87], v[68:71]\n"
    "s_waitcnt vmcnt(8)\n"
    "s_cbranch_vccz .Lfd_qf31\n"
    "s_waitcnt vmcnt(5)\n"
    ".Lfd_qf31:\n"
    "global_load_dwordx4 a[124:127], v6, s[16:17] offset:128 nt\n"
    "v_mfma_f32_16x16x32_bf16 v[72:75], v[26:29], a[88:91], v[72:75]\n"
    "s_waitcnt vmcnt(8)\n"
    "s_cbranch_vccz .Lfd_qf32\n"
    "s_waitcnt vmcnt(5)\n"
    ".Lfd_qf32:\n"
    "global_load_dwordx4 a[128:131], v6, s[16:17] offset:256 nt\n"
    "v_add_u32 v6, s35, v6\n"
    "v_mfma_f32_16x16x32_bf16 v[76:79], v[26:29], a[92:95], v[76:79]\n"
    "v_perm_b32 v22, v60, v60, v10\n"
    "v_perm_b32 v23, v60, v60, v11\n"
    "v_perm_b32 v24, v60, v60, v12\n"
    "v_perm_b32 v25, v60, v60, v13\n"
    "s_memrealtime s[98:99]\n"
    "s_waitcnt vmcnt(8)\n"
    "s_waitcnt lgkmcnt(0)\n"
    "s_sub_u32 s98, s98, s96\n"
    "s_cmpk_lt_u32 s98, 326\n"
    "s_cselect_b64 vcc, -1, 0\n"
    "s_cbranch_vccz .Lfd_qf33\n"
    "s_waitcnt vmcnt(5)\n"
    ".Lfd_qf33:\n"
    "global_load_dwordx4 a[132:135], v6, s[16:17] nt\n"
    "v_mfma_f32_16x16x32_bf16 v[68:71], v[22:25], a[96:99], v[68:71]\n"
    "s_waitcnt vmcnt(8)\n"
    "s_cbranch_vccz .Lfd_qf34\n"
    "s_waitcnt vmcnt(5)\n"
    ".Lfd_qf34:\n"
    "global_load_dwordx4 a[136:139], v6, s[16:17] offset:128 nt\n"
    "v_mfma_f32_16x16x32_bf16 v[72:75], v[22:25], a[100:103], v[72:75]\n"
    "s_waitcnt vmcnt(8)\n"
    "s_cbranch_vccz .Lfd_qf35\n"
    "s_waitcnt vmcnt(5)\n"
    ".Lfd_qf35:\n"
    "global_load_dwordx4 a[140:143], v6, s[16:17] offset:256 nt\n"
    "v_add_u32 v6, s35, v6\n"
    "v_mfma_f32_16x16x32_bf16 v[76:79], v[22:25], a[104:107], v[76:79]\n"
    "v_perm_b32 v26, v52, v52, v10\n"
    "v_perm_b32 v27, v52, v52, v11\n"
    "v_perm_b32 v28, v52, v52, v12\n"
    "v_perm_b32 v29, v52, v52, v13\n"
    "s_waitcnt vmcnt(8)\n"
    "s_cbranch_vccz .Lfd_qf36\n"
    "s_waitcnt vmcnt(5)\n"
    ".Lfd_qf36:\n"
    "global_load_dwordx4 a[144:147], v6, s[16:17] nt\n"
    "v_mfma_f32_16x16x32_bf16 v[80:83], v[26:29], a[108:111], 0\n"
    "s_waitcnt vmcnt(8)\n"
    "s_cbranch_vccz .Lfd_qf37\n"
    "s_waitcnt vmcnt(5)\n"
    ".Lfd_qf37:\n"
    "global_load_dwordx4 a[148:151], v6, s[16:17] offset:128 nt\n"
    "v_mfma_f32_16x16x32_bf16 v[84:87], v[26:29], a[112:115], 0\n"
    "s_memrealtime s[98:99]\n"
    "s_waitcnt vmcnt(8)\n"
    "s_waitcnt lgkmcnt(0)\n"
    "s_sub_u32 s98, s98, s96\n"
    "s_cmpk_lt_u32 s98, 380\n"
    "s_cselect_b64 vcc, -1, 0\n"
    "s_cbranch_vccz .Lfd_qf38\n"
    "s_waitcnt vmcnt(5)\n"
    ".Lfd_qf38:\n"
    "global_load_dwordx4 a[152:155], v6, s[16:17] offset:256 nt\n"
    "v_add_u32 v6, s35, v6\n"
    "v_mfma_f32_16x16x32_bf16 v[88:91], v[26:29], a[116:119], 0\n"
    "v_perm_b32 v22, v53, v53, v10\n"
    "v_perm_b32 v23, v53, v53, v11\n"
    "v_perm_b32 v24, v53, v53, v12\n"
    "v_perm_b32 v25, v53, v53, v13\n"
    "s_waitcnt vmcnt(8)\n"
    "s_cbranch_vccz .Lfd_qf39\n"
    "s_waitcnt vmcnt(5)\n"
    ".Lfd_qf39:\n"
    "global_load_dwordx4 a[156:159], v6, s[16:17] nt\n"
    "v_mfma_f32_16x16x32_bf16 v[80:83], v[22:25], a[120:123], v[80:83]\n"
    "s_waitcnt vmcnt(8)\n"
    "s_cbranch_vccz .Lfd_qf40\n"
    "s_waitcnt vmcnt(5)\n"
    ".Lfd_qf40:\n"
    "global_load_dwordx4 a[160:163], v6, s[16:17] offset:128 nt\n"
    "v_mfma_f32_16x16x32_bf16 v[84:87], v[22:25], a[124:127], v[84:87]\n"
    "s_waitcnt vmcnt(8)\n"
    "s_cbranch_vccz .Lfd_qf41\n"
    "s_waitcnt vmcnt(5)\n"
    ".Lfd_qf41:\n"
    "global_load_dwordx4 a[164:167], v6, s[16:17] offset:256 nt\n"
    "v_add_u32 v6, s35, v6\n"
    "v_mfma_f32_16x16x32_bf16 v[88:91], v[22:25], a[128:131], v[88:91]\n"
    "v_perm_b32 v26, v54, v54, v10\n"
    "v_perm_b32 v27, v54, v54, v11\n"
    "v_perm_b32 v28, v54, v54, v12\n"
    "v_perm_b32 v29, v54, v54, v13\n"
    "s_waitcnt vmcnt(8)\n"
    "s_cbranch_vccz .Lfd_qf42\n"
    "s_waitcnt vmcnt(5)\n"
    ".Lfd_qf42:\n"
    "global_load_dwordx4 a[168:171], v6, s[16:17] nt\n"
    "v_mfma_f32_16x16x32_bf16 v[80:83], v[26:29], a[132:135], v[80:83]\n"
    "s_memrealtime s[98:99]\n"
    "s_waitcnt vmcnt(8)\n"
    "s_waitcnt lgkmcnt(0)\n"
    "s_sub_u32 s98, s98, s96\n"
    "s_cmpk_lt_u32 s98, 434\n"
    "s_cselect_b64 vcc, -1, 0\n"
    "s_cbranch_vccz .Lfd_qf43\n"
    "s_waitcnt vmcnt(5)\n"
    ".Lfd_qf43:\n"
    "global_load_dwordx4 a[172:175], v6, s[16:17] offset:128 nt\n"
    "v_mfma_f32_16x16x32_bf16 v[84:87], v[26:29], a[136:139], v[84:87]\n"
    "s_waitcnt vmcnt(8)\n"
    "s_cbranch_vccz .Lfd_qf44\n"
    "s_waitcnt vmcnt(5)\n"
    ".Lfd_qf44:\n"
    "global_load_dwordx4 a[176:179], v6, s[16:17] offset:256 nt\n"
    "v_add_u32 v6, s35, v6\n"
    "v_mfma_f32_16x16x32_bf16 v[88:91], v[26:29], a[140:143], v[88:91]\n"
    "v_perm_b32 v22, v55, v55, v10\n"
    "v_perm_b32 v23, v55, v55, v11\n"
    "v_perm_b32 v24, v55, v55, v12\n"
    "v_perm_b32 v25, v55, v55, v13\n"
    "s_waitcnt vmcnt(8)\n"
    "s_cbranch_vccz .Lfd_qf45\n"
    "s_waitcnt vmcnt(5)\n"
    ".Lfd_qf45:\n"
    "global_load_dwordx4 a[180:183], v6, s[16:17] nt\n"
    "v_mfma_f32_16x16x32_bf16 v[80:83], v[22:25], a[144:147], v[80:83]\n"
    "s_waitcnt vmcnt(8)\n"
    "s_cbranch_vccz .Lfd_qf46\n"
    "s_waitcnt vmcnt(5)\n"
    ".Lfd_qf46:\n"
    "global_load_dwordx4 a[184:187], v6, s[16:17] offset:128 nt\n"
    "v_mfma_f32_16x16x32_bf16 v[84:87], v[22:25], a[148:151], v[84:87]\n"
    "s_waitcnt vmcnt(8)\n"
    "s_cbranch_vccz .Lfd_qf47\n"
    "s_waitcnt vmcnt(5)\n"
    ".Lfd_qf47:\n"
    "global_load_dwordx4 a[188:191], v6, s[16:17] offset:256 nt\n"
    "v_add_u32 v6, s35, v6\n"
    "v_mfma_f32_16x16x32_bf16 v[88:91], v[22:25], a[152:155], v[88:91]\n"
    "v_perm_b32 v26, v56, v56, v10\n"
    "v_perm_b32 v27, v56, v56, v11\n"
    "v_perm_b32 v28, v56, v56, v12\n"
    "v_perm_b32 v29, v56, v56, v13\n"
    "s_memrealtime s[98:99]\n"
    "s_waitcnt vmcnt(8)\n"
    "s_waitcnt lgkmcnt(0)\n"
    "s_sub_u32 s98, s98, s96\n"
    "s_cmpk_lt_u32 s98, 488\n"
    "s_cselect_b64 vcc, -1, 0\n"
    "s_cbranch_vccz .Lfd_qf48\n"
    "s_waitcnt vmcnt(5)\n"
    ".Lfd_qf48:\n"
    "global_load_dwordx4 a[192:195], v6, s[16:17] nt\n"
    "v_mfma_f32_16x16x32_bf16 v[80:83], v[26:29], a[156:159], v[80:83]\n"
    "s_waitcnt vmcnt(8)\n"
    "s_cbranch_vccz .Lfd_qf49\n"
    "s_waitcnt vmcnt(5)\n"
    ".Lfd_qf49:\n"
    "global_load_dwordx4 a[196:199], v6, s[16:17] offset:128 nt\n"
    "v_mfma_f32_16x16x32_bf16 v[84:87], v[26:29], a[160:163], v[84:87]\n"
    "s_waitcnt vmcnt(8)\n"
    "s_cbranch_vccz .Lfd_qf50\n"
    "s_waitcnt vmcnt(5)\n"
    ".Lfd_qf50:\n"
    "global_load_dwordx4 a[200:203], v6, s[16:17] offset:256 nt\n"
    "v_add_u32 v6, s35, v6\n"
    "v_mfma_f32_16x16x32_bf16 v[88:91], v[26:29], a[164:167], v[88:91]\n"
    "v_perm_b32 v22, v57, v57, v10\n"
    "v_perm_b32 v23, v57, v57, v11\n"
    "v_perm_b32 v24, v57, v57, v12\n"
    "v_perm_b32 v25, v57, v57, v13\n"
    "s_waitcnt vmcnt(8)\n"
    "s_cbranch_vccz .Lfd_qf51\n"
    "s_waitcnt vmcnt(5)\n"
    ".Lfd_qf51:\n"
    "global_load_dwordx4 a[204:207], v6, s[16:17] nt\n"
    "v_mfma_f32_16x16x32_bf16 v[80:83], v[22:25], a[168:171], v[80:83]\n"
    "s_waitcnt vmcnt(8)\n"
    "s_cbranch_vccz .Lfd_qf52\n"
    "s_waitcnt vmcnt(5)\n"
    ".Lfd_qf52:\n"
    "global_load_dwordx4 a[208:211], v6, s[16:17] offset:128 nt\n"
    "v_mfma_f32_16x16x32_bf16 v[84:87], v[22:25], a[172:175], v[84:87]\n"
    "s_memrealtime s[98:99]\n"
    "s_waitcnt vmcnt(8)\n"
    "s_waitcnt lgkmcnt(0)\n"
    "s_sub_u32 s98, s98, s96\n"
    "s_cmpk_lt_u32 s98, 542\n"
    "s_cselect_b64 vcc, -1, 0\n"
    "s_cbranch_vccz .Lfd_qf53\n"
    "s_waitcnt vmcnt(5)\n"
    ".Lfd_qf53:\n"
    "global_load_dwordx4 a[212:215], v6, s[16:17] offset:256 nt\n"
    "v_mfma_f32_16x16x32_bf16 v[88:91], v[22:25], a[176:179], v[88:91]\n"
    "v_perm_b32 v26, v58, v58, v10\n"
    "v_perm_b32 v27, v58, v58, v11\n"
    "v_perm_b32 v28, v58, v58, v12\n"
    "v_perm_b32 v29, v58, v58, v13\n"
    "s_memrealtime s[98:99]\n"
    "s_waitcnt vmcnt(8)\n"
    "s_waitcnt lgkmcnt(0)\n"
    "s_sub_u32 s98, s98, s96\n"
    "s_cmpk_lt_u32 s98, 553\n"
    "s_cselect_b64 vcc, -1, 0\n"
    "s_cbranch_vccz .Lfd_qf54\n"
    "s_waitcnt vmcnt(5)\n"
    ".Lfd_qf54:\n"
    "global_load_dwordx4 a[216:219], v5, s[18:19] nt\n"
    "v_mfma_f32_16x16x32_bf16 v[80:83], v[26:29], a[180:183], v[80:83]\n"
    "s_waitcnt vmcnt(8)\n"
    "s_cbranch_vccz .Lfd_qf55\n"
    "s_waitcnt vmcnt(5)\n"
    ".Lfd_qf55:\n"
    "global_load_dwordx4 a[220:223], v5, s[18:19] offset:128 nt\n"
    "v_mfma_f32_16x16x32_bf16 v[84:87], v[26:29], a[184:187], v[84:87]\n"
    "s_waitcnt vmcnt(8)\n"
    "s_cbranch_vccz .Lfd_qf56\n"
    "s_waitcnt vmcnt(5)\n"
    ".Lfd_qf56:\n"
    "global_load_dwordx4 a[224:227], v5, s[18:19] offset:256 nt\n"
    "v_add_u32 v5, s37, v5\n"
    "v_mfma_f32_16x16x32_bf16 v[88:91], v[26:29], a[188:191], v[88:91]\n"
    "v_perm_b32 v22, v59, v59, v10\n"
    "v_perm_b32 v23, v59, v59, v11\n"
    "v_perm_b32 v24, v59, v59, v12\n"
    "v_perm_b32 v25, v59, v59, v13\n"
    "s_waitcnt vmcnt(8)\n"
    "s_cbranch_vccz .Lfd_qf57\n"
    "s_waitcnt vmcnt(5)\n"
    ".Lfd_qf57:\n"
    "global_load_dwordx4 a[228:231], v5, s[18:19] nt\n"
    "v_mfma_f32_16x16x32_bf16 v[80:83], v[22:25], a[192:195], v[80:83]\n"
    "s_waitcnt vmcnt(8)\n"
    "s_cbranch_vccz .Lfd_qf58\n"
    "s_waitcnt vmcnt(5)\n"
    ".Lfd_qf58:\n"
    "global_load_dwordx4 a[232:235], v5, s[18:19] offset:128 nt\n"
    "v_mfma_f32_16x16x32_bf16 v[84:87], v[22:25], a[196:199], v[84:87]\n"
    "s_memrealtime s[98:99]\n"
    "s_waitcnt vmcnt(8)\n"
    "s_waitcnt lgkmcnt(0)\n"
    "s_sub_u32 s98, s98, s96\n"
    "s_cmpk_lt_u32 s98, 607\n"
    "s_cselect_b64 vcc, -1, 0\n"
    "s_cbranch_vccz .Lfd_qf59\n"
    "s_waitcnt vmcnt(5)\n"
    ".Lfd_qf59:\n"
    "global_load_dwordx4 a[236:239], v5, s[18:19] offset:256 nt\n"
    "v_add_u32 v5, s37, v5\n"
    "v_mfma_f32_16x16x32_bf16 v[88:91], v[22:25], a[200:203], v[88:91]\n"
    "v_perm_b32 v26, v60, v60, v10\n"
    "v_perm_b32 v27, v60, v60, v11\n"
    "v_perm_b32 v28, v60, v60, v12\n"
    "v_perm_b32 v29, v60, v60, v13\n"
    "s_waitcnt vmcnt(8)\n"
    "global_load_dwordx4 a[240:243], v5, s[18:19] nt\n"
    "v_mfma_f32_16x16x32_bf16 v[80:83], v[26:29], a[204:207], v[80:83]\n"
    "s_waitcnt vmcnt(8)\n"
    "global_load_dwordx4 a[244:247], v5, s[18:19] offset:128 nt\n"
    "v_mfma_f32_16x16x32_bf16 v[84:87], v[26:29], a[208:211], v[84:87]\n"
    "s_waitcnt vmcnt(8)\n"
    "global_load_dwordx4 a[248:251], v5, s[18:19] offset:256 nt\n"
    "v_add_u32 v5, s37, v5\n"
    "v_mfma_f32_16x16x32_bf16 v[88:91], v[26:29], a[212:215], v[88:91]\n"
    "s_nop 0\n"
    "v_permlane32_swap_b32 v68, v80\n"
    "v_permlane32_swap_b32 v69, v81\n"
    "v_permlane32_swap_b32 v70, v82\n"
    "v_permlane32_swap_b32 v71, v83\n"
    "v_permlane32_swap_b32 v72, v84\n"
    "v_permlane32_swap_b32 v73, v85\n"
    "v_permlane32_swap_b32 v74, v86\n"
    "v_permlane32_swap_b32 v75, v87\n"
    "v_permlane32_swap_b32 v76, v88\n"
    "v_permlane32_swap_b32 v77, v89\n"
    "v_permlane32_swap_b32 v78, v90\n"
    "v_permlane32_swap_b32 v79, v91\n"
    "v_add_f32_dpp v68, v80, v68 row_ror:8 row_mask:0xf bank_mask:0xf\n"
    "v_add_f32_dpp v69, v81, v69 row_ror:8 row_mask:0xf bank_mask:0xf\n"
    "v_add_f32_dpp v70, v82, v70 row_ror:8 row_mask:0xf bank_mask:0xf\n"
    "v_add_f32_dpp v71, v83, v71 row_ror:8 row_mask:0xf bank_mask:0xf\n"
    "v_add_f32_dpp v72, v84, v72 row_ror:8 row_mask:0xf bank_mask:0xf\n"
    "v_add_f32_dpp v73, v85, v73 row_ror:8 row_mask:0xf bank_mask:0xf\n"
    "v_add_f32_dpp v74, v86, v74 row_ror:8 row_mask:0xf bank_mask:0xf\n"
    "v_add_f32_dpp v75, v87, v75 row_ror:8 row_mask:0xf bank_mask:0xf\n"
    "v_add_f32_dpp v76, v88, v76 row_ror:8 row_mask:0xf bank_mask:0xf\n"
    "v_add_f32_dpp v77, v89, v77 row_ror:8 row_mask:0xf bank_mask:0xf\n"
    "v_add_f32_dpp v78, v90, v78 row_ror:8 row_mask:0xf bank_mask:0xf\n"
    "v_add_f32_dpp v79, v91, v79 row_ror:8 row_mask:0xf bank_mask:0xf\n"
    "v_add_u32 v68, 0x200, v68\n"
    "v_bfi_b32 v68, v97, s24, v68\n"
    "v_add_u32 v69, 0x200, v69\n"
    "v_bfi_b32 v69, v97, s25, v69\n"
    "v_add_u32 v70, 0x200, v70\n"
    "v_bfi_b32 v70, v97, s26, v70\n"
    "v_add_u32 v71, 0x200, v71\n"
    "v_bfi_b32 v71, v97, s27, v71\n"
    "v_add_u32 v72, 0x200, v72\n"
    "v_bfi_b32 v72, v97, s24, v72\n"
    "v_add_u32 v73, 0x200, v73\n"
    "v_bfi_b32 v73, v97, s25, v73\n"
    "v_add_u32 v74, 0x200, v74\n"
    "v_bfi_b32 v74, v97, s26, v74\n"
    "v_add_u32 v75, 0x200, v75\n"
    "v_bfi_b32 v75, v97, s27, v75\n"
    "v_add_u32 v76, 0x200, v76\n"
    "v_bfi_b32 v76, v97, s24, v76\n"
    "v_add_u32 v77, 0x200, v77\n"
    "v_bfi_b32 v77, v97, s25, v77\n"
    "v_add_u32 v78, 0x200, v78\n"
    "v_bfi_b32 v78, v97, s26, v78\n"
    "v_add_u32 v79, 0x200, v79\n"
    "v_bfi_b32 v79, v97, s27, v79\n"
    "s_mov_b64 exec, s[46:47]\n"
    "global_store_dwordx4 v67, v[68:71], s[22:23] offset:0 sc1\n"
    "global_store_dwordx4 v67, v[72:75], s[22:23] offset:512 sc1\n"
    "global_store_dwordx4 v67, v[76:79], s[22:23] offset:1024 sc1\n"
    "s_mov_b64 exec, -1\n"
    "s_barrier\n"
    "s_waitcnt vmcnt(8)\n"
    "global_load_dwordx4 a[252:255], v5, s[18:19] nt\n"
    "s_waitcnt vmcnt(8)\n"
    "global_load_dwordx4 v[100:103], v5, s[18:19] offset:128 nt\n"
    "s_waitcnt vmcnt(8)\n"
    "global_load_dwordx4 v[104:107], v5, s[18:19] offset:256 nt\n"
    "v_add_u32 v5, s37, v5\n"
    "s_waitcnt vmcnt(8)\n"
    "global_load_dwordx4 v[108:111], v5, s[18:19] nt\n"
    "s_waitcnt vmcnt(8)\n"
    "global_load_dwordx4 v[112:115], v5, s[18:19] offset:128 nt\n"
    "s_waitcnt vmcnt(8)\n"
    "global_load_dwordx4 v[116:119], v5, s[18:19] offset:256 nt\n"
    "v_add_u32 v5, s37, v5\n"
    "s_waitcnt vmcnt(8)\n"
    "global_load_dwordx4 v[120:123], v5, s[18:19] nt\n"
    "s_waitcnt vmcnt(8)\n"
    "global_load_dwordx4 v[124:127], v5, s[18:19] offset:128 nt\n"
    "s_waitcnt vmcnt(8)\n"
    "global_load_dwordx4 v[128:131], v5, s[18:19] offset:256 nt\n"
    "v_add_u32 v5, s37, v5\n"
    "s_waitcnt vmcnt(8)\n"
    "global_load_dwordx4 v[132:135], v5, s[18:19] nt\n"
    "s_waitcnt vmcnt(8)\n"
    "global_load_dwordx4 v[136:139], v5, s[18:19] offset:128 nt\n"
    "s_waitcnt vmcnt(8)\n"
    "global_load_dwordx4 v[140:143], v5, s[18:19] offset:256 nt\n"
    "v_add_u32 v5, s37, v5\n"
    "s_waitcnt vmcnt(8)\n"
    "global_load_dwordx4 v[144:147], v5, s[18:19] nt\n"
    "s_waitcnt vmcnt(8)\n"
    "global_load_dwordx4 v[148:151], v5, s[18:19] offset:128 nt\n"
    "s_waitcnt vmcnt(8)\n"
    "global_load_dwordx4 v[152:155], v5, s[18:19] offset:256 nt\n"
    "v_add_u32 v5, s37, v5\n"
    "s_waitcnt vmcnt(8)\n"
    "global_load_dwordx4 v[156:159], v5, s[18:19] nt\n"
    "s_waitcnt vmcnt(8)\n"
    "global_load_dwordx4 v[160:163], v5, s[18:19] offset:128 nt\n"
    "s_waitcnt vmcnt(8)\n"
    "global_load_dwordx4 v[164:167], v5, s[18:19] offset:256 nt\n"
    "s_barrier\n"
    "ds_read_u16 v52, v49 offset:1664\n"
    "ds_read_u16 v53, v49 offset:1680\n"
    "ds_read_u16 v54, v49 offset:1696\n"
    "ds_read_u16 v55, v49 offset:1712\n"
    "ds_read_u16 v56, v49 offset:1728\n"
    "ds_read_u16 v57, v49 offset:1744\n"
    "ds_read_u16 v58, v49 offset:1760\n"
    "ds_read_u16 v59, v49 offset:1776\n"
    "ds_read_u16 v60, v49 offset:1792\n"
    "s_waitcnt lgkmcnt(0)\n"
    "v_perm_b32 v22, v52, v52, v10\n"
    "v_perm_b32 v23, v52, v52, v11\n"
    "v_perm_b32 v24, v52, v52, v12\n"
    "v_perm_b32 v25, v52, v52, v13\n"
    "s_nop 1\n"
    "v_mfma_f32_16x16x32_bf16 v[30:33], v[22:25], a[216:219], 0\n"
    "v_mfma_f32_16x16x32_bf16 v[34:37], v[22:25], a[220:223], 0\n"
    "v_mfma_f32_16x16x32_bf16 v[38:41], v[22:25], a[224:227], 0\n"
    "v_perm_b32 v26, v53, v53, v10\n"
    "v_perm_b32 v27, v53, v53, v11\n"
    "v_perm_b32 v28, v53, v53, v12\n"
    "v_perm_b32 v29, v53, v53, v13\n"
    "s_nop 1\n"
    "v_mfma_f32_16x16x32_bf16 v[30:33], v[26:29], a[228:231], v[30:33]\n"
    "v_mfma_f32_16x16x32_bf16 v[34:37], v[26:29], a[232:235], v[34:37]\n"
    "v_mfma_f32_16x16x32_bf16 v[38:41], v[26:29], a[236:239], v[38:41]\n"
    "v_perm_b32 v22, v54, v54, v10\n"
    "v_perm_b32 v23, v54, v54, v11\n"
    "v_perm_b32 v24, v54, v54, v12\n"
    "v_perm_b32 v25, v54, v54, v13\n"
    "s_nop 1\n"
    "v_mfma_f32_16x16x32_bf16 v[30:33], v[22:25], a[240:243], v[30:33]\n"
    "v_mfma_f32_16x16x32_bf16 v[34:37], v[22:25], a[244:247], v[34:37]\n"
    "v_mfma_f32_16x16x32_bf16 v[38:41], v[22:25], a[248:251], v[38:41]\n"
    "v_perm_b32 v26, v55, v55, v10\n"
    "v_perm_b32 v27, v55, v55, v11\n"
    "v_perm_b32 v28, v55, v55, v12\n"
    "v_perm_b32 v29, v55, v55, v13\n"
    "s_nop 1\n"
    "v_mfma_f32_16x16x32_bf16 v[30:33], v[26:29], a[252:255], v[30:33]\n"
    "v_mfma_f32_16x16x32_bf16 v[34:37], v[26:29], v[100:103], v[34:37]\n"
    "v_mfma_f32_16x16x32_bf16 v[38:41], v[26:29], v[104:107], v[38:41]\n"
    "v_perm_b32 v22, v56, v56, v10\n"
    "v_perm_b32 v23, v56, v56, v11\n"
    "v_perm_b32 v24, v56, v56, v12\n"
    "v_perm_b32 v25, v56, v56, v13\n"
    "s_nop 1\n"
    "v_mfma_f32_16x16x32_bf16 v[30:33], v[22:25], v[108:111], v[30:33]\n"
    "v_mfma_f32_16x16x32_bf16 v[34:37], v[22:25], v[112:115], v[34:37]\n"
    "v_mfma_f32_16x16x32_bf16 v[38:41], v[22:25], v[116:119], v[38:41]\n"
    "v_perm_b32 v26, v57, v57, v10\n"
    "v_perm_b32 v27, v57, v57, v11\n"
    "v_perm_b32 v28, v57, v57, v12\n"
    "v_perm_b32 v29, v57, v57, v13\n"
    "s_nop 1\n"
    "v_mfma_f32_16x16x32_bf16 v[30:33], v[26:29], v[120:123], v[30:33]\n"
    "v_mfma_f32_16x16x32_bf16 v[34:37], v[26:29], v[124:127], v[34:37]\n"
    "v_mfma_f32_16x16x32_bf16 v[38:41], v[26:29], v[128:131], v[38:41]\n"
    "v_perm_b32 v22, v58, v58, v10\n"
    "v_perm_b32 v23, v58, v58, v11\n"
    "v_perm_b32 v24, v58, v58, v12\n"
    "v_perm_b32 v25, v58, v58, v13\n"
    "s_waitcnt vmcnt(8)\n"
    "s_nop 0\n"
    "v_mfma_f32_16x16x32_bf16 v[30:33], v[22:25], v[132:135], v[30:33]\n"
    "s_waitcnt vmcnt(7)\n"
    "v_mfma_f32_16x16x32_bf16 v[34:37], v[22:25], v[136:139], v[34:37]\n"
    "s_waitcnt vmcnt(6)\n"
    "v_mfma_f32_16x16x32_bf16 v[38:41], v[22:25], v[140:143], v[38:41]\n"
    "v_perm_b32 v26, v59, v59, v10\n"
    "v_perm_b32 v27, v59, v59, v11\n"
    "v_perm_b32 v28, v59, v59, v12\n"
    "v_perm_b32 v29, v59, v59, v13\n"
    "s_waitcnt vmcnt(5)\n"
    "s_nop 0\n"
    "v_mfma_f32_16x16x32_bf16 v[30:33], v[26:29], v[144:147], v[30:33]\n"
    "s_waitcnt vmcnt(4)\n"
    "v_mfma_f32_16x16x32_bf16 v[34:37], v[26:29], v[148:151], v[34:37]\n"
    "s_waitcnt vmcnt(3)\n"
    "v_mfma_f32_16x16x32_bf16 v[38:41], v[26:29], v[152:155], v[38:41]\n"
    "v_perm_b32 v22, v60, v60, v10\n"
    "v_perm_b32 v23, v60, v60, v11\n"
    "v_perm_b32 v24, v60, v60, v12\n"
    "v_perm_b32 v25, v60, v60, v13\n"
    "s_waitcnt vmcnt(2)\n"
    "s_nop 0\n"
    "v_mfma_f32_16x16x32_bf16 v[30:33], v[22:25], v[156:159], v[30:33]\n"
    "s_waitcnt vmcnt(1)\n"
    "v_mfma_f32_16x16x32_bf16 v[34:37], v[22:25], v[160:163], v[34:37]\n"
    "s_waitcnt vmcnt(0)\n"
    "v_mfma_f32_16x16x32_bf16 v[38:41], v[22:25], v[164:167], v[38:41]\n"
    "s_barrier\n"
    "s_nop 2\n"
    "v_permlane32_swap_b32 v30, v32\n"
    "v_permlane32_swap_b32 v31, v33\n"
    "v_permlane32_swap_b32 v34, v36\n"
    "v_permlane32_swap_b32 v35, v37\n"
    "v_permlane32_swap_b32 v38, v40\n"
    "v_permlane32_swap_b32 v39, v41\n"
    "v_add_f32_dpp v30, v32, v30 row_ror:8 row_mask:0xf bank_mask:0xf\n"
    "v_add_f32_dpp v31, v33, v31 row_ror:8 row_mask:0xf bank_mask:0xf\n"
    "v_add_f32_dpp v34, v36, v34 row_ror:8 row_mask:0xf bank_mask:0xf\n"
    "v_add_f32_dpp v35, v37, v35 row_ror:8 row_mask:0xf bank_mask:0xf\n"
    "v_add_f32_dpp v38, v40, v38 row_ror:8 row_mask:0xf bank_mask:0xf\n"
    "v_add_f32_dpp v39, v41, v39 row_ror:8 row_mask:0xf bank_mask:0xf\n"
    "s_mov_b64 exec, s[46:47]\n"
    "v_add_u32 v30, 0x200, v30\n"
    "v_add_u32 v31, 0x200, v31\n"
    "v_bfi_b32 v30, v97, v95, v30\n"
    "v_bfi_b32 v31, v97, v96, v31\n"
    "global_store_dwordx2 v64, v[30:31], s[22:23] sc1\n"
    "v_add_u32 v34, 0x200, v34\n"
    "v_add_u32 v35, 0x200, v35\n"
    "v_bfi_b32 v34, v97, v95, v34\n"
    "v_bfi_b32 v35, v97, v96, v35\n"
    "global_store_dwordx2 v65, v[34:35], s[22:23] sc1\n"
    "v_add_u32 v38, 0x200, v38\n"
    "v_add_u32 v39, 0x200, v39\n"
    "v_bfi_b32 v38, v97, v95, v38\n"
    "v_bfi_b32 v39, v97, v96, v39\n"
    "global_store_dwordx2 v66, v[38:39], s[22:23] sc1\n"
    "s_branch .Lfd_red\n"
    ".Lfd_a2:\n"
    "v_and_b32 v42, 1, v0\n"
    "v_cmp_eq_u32 vcc, 1, v42\n"
    "v_mov_b32 v95, s24\n"
    "v_mov_b32 v42, s26\n"
    "v_cndmask_b32 v95, v95, v42, vcc\n"
    "v_mov_b32 v96, s25\n"
    "v_mov_b32 v42, s27\n"
    "v_cndmask_b32 v96, v96, v42, vcc\n"
    "v_mov_b32 v97, 0x3ff\n"
    "s_lshl_b32 s34, s39, 15\n"
    "s_lshl_b32 s45, s38, 7\n"
    "s_add_u32 s34, s34, s45\n"
    "s_add_u32 s34, s34, 618496\n"
    "v_lshrrev_b32 v42, 5, v0\n"
    "v_lshl_add_u32 v98, v42, 14, s34\n"
    "v_bfe_u32 v42, v0, 4, 1\n"
    "v_lshl_add_u32 v98, v42, 6, v98\n"
    "v_and_b32 v42, 7, v0\n"
    "v_lshl_add_u32 v98, v42, 3, v98\n"
    "s_mov_b32 s46, 0x00ff00ff\n"
    "s_mov_b32 s47, 0x00ff00ff\n"
    "v_and_b32 v42, 0xf0, v3\n"
    "v_and_b32 v43, 8, v0\n"
    "v_lshl_add_u32 v42, v43, 5, v42\n"
    "v_xor_b32 v43, 0x100, v42\n"
    "s_waitcnt vmcnt(8)\n"
    "global_load_dwordx4 v[136:139], v5, s[6:7] offset:0 nt\n"
    "v_add_u32 v5, s50, v5\n"
    "s_waitcnt vmcnt(8)\n"
    "global_load_dwordx4 v[140:143], v5, s[6:7] offset:0 nt\n"
    "v_add_u32 v5, s50, v5\n"
    "s_waitcnt vmcnt(8)\n"
    "global_load_dwordx4 v[144:147], v5, s[6:7] offset:0 nt\n"
    "v_add_u32 v5, s50, v5\n"
    "s_waitcnt vmcnt(8)\n"
    "global_load_dwordx4 v[148:151], v5, s[6:7] offset:0 nt\n"
    "v_add_u32 v5, s50, v5\n"
    "s_waitcnt vmcnt(8)\n"
    "global_load_dwordx4 v[152:155], v5, s[6:7] offset:0 nt\n"
    "v_add_u32 v5, s50, v5\n"
    "s_memrealtime s[98:99]\n"
    "s_waitcnt vmcnt(8)\n"
    "s_waitcnt lgkmcnt(0)\n"
    "s_sub_u32 s98, s98, s96\n"
    "s_cmpk_lt_u32 s98, 206\n"
    "s_cselect_b64 vcc, -1, 0\n"
    "s_cbranch_vccz .Lfd_qa23\n"
    "s_waitcnt vmcnt(5)\n"
    ".Lfd_qa23:\n"
    "global_load_dwordx4 v[156:159], v5, s[6:7] offset:0 nt\n"
    "v_add_u32 v5, s50, v5\n"
    "s_waitcnt vmcnt(8)\n"
    "s_cbranch_vccz .Lfd_qa24\n"
    "s_waitcnt vmcnt(5)\n"
    ".Lfd_qa24:\n"
    "global_load_dwordx4 v[160:163], v5, s[6:7] offset:0 nt\n"
    "v_add_u32 v5, s50, v5\n"
    "s_waitcnt vmcnt(8)\n"
    "s_cbranch_vccz .Lfd_qa25\n"
    "s_waitcnt vmcnt(5)\n"
    ".Lfd_qa25:\n"
    "global_load_dwordx4 v[164:167], v5, s[6:7] offset:0 nt\n"
    "v_add_u32 v5, s50, v5\n"
    "s_waitcnt vmcnt(8)\n"
    "s_cbranch_vccz .Lfd_qa26\n"
    "s_waitcnt vmcnt(5)\n"
    ".Lfd_qa26:\n"
    "global_load_dwordx4 v[168:171], v5, s[6:7] offset:0 nt\n"
    "v_add_u32 v5, s50, v5\n"
    "s_waitcnt vmcnt(8)\n"
    "s_cbranch_vccz .Lfd_qa27\n"
    "s_waitcnt vmcnt(5)\n"
    ".Lfd_qa27:\n"
    "global_load_dwordx4 v[172:175], v5, s[6:7] offset:0 nt\n"
    "v_add_u32 v5, s50, v5\n"
    "s_memrealtime s[98:99]\n"
    "s_waitcnt vmcnt(8)\n"
    "s_waitcnt lgkmcnt(0)\n"
    "s_sub_u32 s98, s98, s96\n"
    "s_cmpk_lt_u32 s98, 258\n"
    "s_cselect_b64 vcc, -1, 0\n"
    "s_cbranch_vccz .Lfd_qa28\n"
    "s_waitcnt vmcnt(5)\n"
    ".Lfd_qa28:\n"
    "global_load_dwordx4 v[176:179], v5, s[6:7] offset:0 nt\n"
    "v_add_u32 v5, s50, v5\n"
    "s_waitcnt vmcnt(8)\n"
    "s_cbranch_vccz .Lfd_qa29\n"
    "s_waitcnt vmcnt(5)\n"
    ".Lfd_qa29:\n"
    "global_load_dwordx4 v[180:183], v5, s[6:7] offset:0 nt\n"
    "v_add_u32 v5, s50, v5\n"
    "s_waitcnt vmcnt(8)\n"
    "s_cbranch_vccz .Lfd_qa30\n"
    "s_waitcnt vmcnt(5)\n"
    ".Lfd_qa30:\n"
    "global_load_dwordx4 v[184:187], v5, s[6:7] offset:0 nt\n"
    "v_add_u32 v5, s50, v5\n"
    "s_waitcnt vmcnt(8)\n"
    "s_cbranch_vccz .Lfd_qa31\n"
    "s_waitcnt vmcnt(5)\n"
    ".Lfd_qa31:\n"
    "global_load_dwordx4 v[188:191], v5, s[6:7] offset:0 nt\n"
    "v_add_u32 v5, s50, v5\n"
    "s_waitcnt vmcnt(8)\n"
    "s_cbranch_vccz .Lfd_qa32\n"
    "s_waitcnt vmcnt(5)\n"
    ".Lfd_qa32:\n"
    "global_load_dwordx4 v[192:195], v5, s[6:7] offset:0 nt\n"
    "v_add_u32 v5, s50, v5\n"
    "s_memrealtime s[98:99]\n"
    "s_waitcnt vmcnt(8)\n"
    "s_waitcnt lgkmcnt(0)\n"
    "s_sub_u32 s98, s98, s96\n"
    "s_cmpk_lt_u32 s98, 309\n"
    "s_cselect_b64 vcc, -1, 0\n"
    "s_cbranch_vccz .Lfd_qa33\n"
    "s_waitcnt vmcnt(5)\n"
    ".Lfd_qa33:\n"
    "global_load_dwordx4 v[196:199], v5, s[6:7] offset:0 nt\n"
    "v_add_u32 v5, s50, v5\n"
    "s_waitcnt vmcnt(8)\n"
    "s_cbranch_vccz .Lfd_qa34\n"
    "s_waitcnt vmcnt(5)\n"
    ".Lfd_qa34:\n"
    "global_load_dwordx4 v[200:203], v5, s[6:7] offset:0 nt\n"
    "v_add_u32 v5, s50, v5\n"
    "s_waitcnt vmcnt(8)\n"
    "s_cbranch_vccz .Lfd_qa35\n"
    "s_waitcnt vmcnt(5)\n"
    ".Lfd_qa35:\n"
    "global_load_dwordx4 v[204:207], v5, s[6:7] offset:0 nt\n"
    "v_add_u32 v5, s50, v5\n"
    "s_waitcnt vmcnt(8)\n"
    "s_cbranch_vccz .Lfd_qa36\n"
    "s_waitcnt vmcnt(5)\n"
    ".Lfd_qa36:\n"
    "global_load_dwordx4 v[208:211], v5, s[6:7] offset:0 nt\n"
    "s_waitcnt vmcnt(8)\n"
    "s_cbranch_vccz .Lfd_qa37\n"
    "s_waitcnt vmcnt(5)\n"
    ".Lfd_qa37:\n"
    "global_load_dwordx4 a[0:3], v6, s[8:9] offset:0 nt\n"
    "v_add_u32 v6, s50, v6\n"
    "s_memrealtime s[98:99]\n"
    "s_waitcnt vmcnt(8)\n"
    "s_waitcnt lgkmcnt(0)\n"
    "s_sub_u32 s98, s98, s96\n"
    "s_cmpk_lt_u32 s98, 361\n"
    "s_cselect_b64 vcc, -1, 0\n"
    "s_cbranch_vccz .Lfd_qa38\n"
    "s_waitcnt vmcnt(5)\n"
    ".Lfd_qa38:\n"
    "global_load_dwordx4 a[4:7], v6, s[8:9] offset:0 nt\n"
    "v_add_u32 v6, s50, v6\n"
    "s_waitcnt vmcnt(8)\n"
    "s_cbranch_vccz .Lfd_qa39\n"
    "s_waitcnt vmcnt(5)\n"
    ".Lfd_qa39:\n"
    "global_load_dwordx4 a[8:11], v6, s[8:9] offset:0 nt\n"
    "v_add_u32 v6, s50, v6\n"
    "s_waitcnt vmcnt(8)\n"
    "s_cbranch_vccz .Lfd_qa40\n"
    "s_waitcnt vmcnt(5)\n"
    ".Lfd_qa40:\n"
    "global_load_dwordx4 a[12:15], v6, s[8:9] offset:0 nt\n"
    "v_add_u32 v6, s50, v6\n"
    "s_waitcnt vmcnt(8)\n"
    "s_cbranch_vccz .Lfd_qa41\n"
    "s_waitcnt vmcnt(5)\n"
    ".Lfd_qa41:\n"
    "global_load_dwordx4 a[16:19], v6, s[8:9] offset:0 nt\n"
    "v_add_u32 v6, s50, v6\n"
    "s_barrier\n"
    "ds_read_b128 v[34:37], v42 offset:1152\n"
    "ds_read_b128 v[38:41], v43 offset:1152\n"
    "s_waitcnt vmcnt(8)\n"
    "s_cbranch_vccz .Lfd_qa42\n"
    "s_waitcnt vmcnt(5)\n"
    ".Lfd_qa42:\n"
    "global_load_dwordx4 a[20:23], v6, s[8:9] offset:0 nt\n"
    "v_add_u32 v6, s50, v6\n"
    "s_waitcnt lgkmcnt(0)\n"
    "v_dot2_f32_bf16 v44, v100, v38, 0\n"
    "v_dot2_f32_bf16 v50, v100, v34, 0\n"
    "v_dot2_f32_bf16 v44, v101, v39, v44\n"
    "v_dot2_f32_bf16 v50, v101, v35, v50\n"
    "v_dot2_f32_bf16 v44, v102, v40, v44\n"
    "v_dot2_f32_bf16 v50, v102, v36, v50\n"
    "v_dot2_f32_bf16 v44, v103, v41, v44\n"
    "v_dot2_f32_bf16 v50, v103, v37, v50\n"
    "v_dot2_f32_bf16 v45, v104, v38, 0\n"
    "v_dot2_f32_bf16 v51, v104, v34, 0\n"
    "v_dot2_f32_bf16 v45, v105, v39, v45\n"
    "v_add_f32_dpp v50, v44, v50 row_ror:8 row_mask:0xf bank_mask:0xf\n"
    "v_dot2_f32_bf16 v51, v105, v35, v51\n"
    "v_dot2_f32_bf16 v45, v106, v40, v45\n"
    "v_add_f32_dpp v50, v50, v50 row_half_mirror row_mask:0xf bank_mask:0xf\n"
    "v_dot2_f32_bf16 v51, v106, v36, v51\n"
    "v_dot2_f32_bf16 v45, v107, v41, v45\n"
    "v_add_f32_dpp v50, v50, v50 quad_perm:[2,3,0,1] row_mask:0xf bank_mask:0xf\n"
    "v_dot2_f32_bf16 v51, v107, v37, v51\n"
    "v_dot2_f32_bf16 v44, v108, v38, 0\n"
    "v_add_f32_dpp v50, v50, v50 quad_perm:[1,0,3,2] row_mask:0xf bank_mask:0xf\n"
    "v_dot2_f32_bf16 v52, v108, v34, 0\n"
    "v_dot2_f32_bf16 v44, v109, v39, v44\n"
    "v_add_f32_dpp v51, v45, v51 row_ror:8 row_mask:0xf bank_mask:0xf\n"
    "v_dot2_f32_bf16 v52, v109, v35, v52\n"
    "v_dot2_f32_bf16 v44, v110, v40, v44\n"
    "v_add_f32_dpp v51, v51, v51 row_half_mirror row_mask:0xf bank_mask:0xf\n"
    "v_dot2_f32_bf16 v52, v110, v36, v52\n"
    "v_dot2_f32_bf16 v44, v111, v41, v44\n"
    "v_add_f32_dpp v51, v51, v51 quad_perm:[2,3,0,1] row_mask:0xf bank_mask:0xf\n"
    "v_dot2_f32_bf16 v52, v111, v37, v52\n"
    "s_memrealtime s[98:99]\n"
    "s_waitcnt vmcnt(8)\n"
    "s_waitcnt lgkmcnt(0)\n"
    "s_sub_u32 s98, s98, s96\n"
    "s_cmpk_lt_u32 s98, 412\n"
    "s_cselect_b64 vcc, -1, 0\n"
    "s_cbranch_vccz .Lfd_qa43\n"
    "s_waitcnt vmcnt(5)\n"
    ".Lfd_qa43:\n"
    "global_load_dwordx4 a[24:27], v6, s[8:9] offset:0 nt\n"
    "v_add_u32 v6, s50, v6\n"
    "v_dot2_f32_bf16 v45, v112, v38, 0\n"
    "v_add_f32_dpp v51, v51, v51 quad_perm:[1,0,3,2] row_mask:0xf bank_mask:0xf\n"
    "v_dot2_f32_bf16 v53, v112, v34, 0\n"
    "v_dot2_f32_bf16 v45, v113, v39, v45\n"
    "v_add_f32_dpp v52, v44, v52 row_ror:8 row_mask:0xf bank_mask:0xf\n"
    "v_dot2_f32_bf16 v53, v113, v35, v53\n"
    "v_dot2_f32_bf16 v45, v114, v40, v45\n"
    "v_add_f32_dpp v52, v52, v52 row_half_mirror row_mask:0xf bank_mask:0xf\n"
    "v_dot2_f32_bf16 v53, v114, v36, v53\n"
    "v_dot2_f32_bf16 v45, v115, v41, v45\n"
    "v_add_f32_dpp v52, v52, v52 quad_perm:[2,3,0,1] row_mask:0xf bank_mask:0xf\n"
    "v_dot2_f32_bf16 v53, v115, v37, v53\n"
    "v_dot2_f32_bf16 v44, v116, v38, 0\n"
    "v_add_f32_dpp v52, v52, v52 quad_perm:[1,0,3,2] row_mask:0xf bank_mask:0xf\n"
    "v_dot2_f32_bf16 v54, v116, v34, 0\n"
    "v_dot2_f32_bf16 v44, v117, v39, v44\n"
    "v_add_f32_dpp v53, v45, v53 row_ror:8 row_mask:0xf bank_mask:0xf\n"
    "v_dot2_f32_bf16 v54, v117, v35, v54\n"
    "v_dot2_f32_bf16 v44, v118, v40, v44\n"
    "v_add_f32_dpp v53, v53, v53 row_half_mirror row_mask:0xf bank_mask:0xf\n"
    "v_dot2_f32_bf16 v54, v118, v36, v54\n"
    "v_dot2_f32_bf16 v44, v119, v41, v44\n"
    "v_add_f32_dpp v53, v53, v53 quad_perm:[2,3,0,1] row_mask:0xf bank_mask:0xf\n"
    "v_dot2_f32_bf16 v54, v119, v37, v54\n"
    "v_dot2_f32_bf16 v45, v120, v38, 0\n"
    "v_add_f32_dpp v53, v53, v53 quad_perm:[1,0,3,2] row_mask:0xf bank_mask:0xf\n"
    "v_dot2_f32_bf16 v55, v120, v34, 0\n"
    "v_dot2_f32_bf16 v45, v121, v39, v45\n"
    "v_add_f32_dpp v54, v44, v54 row_ror:8 row_mask:0xf bank_mask:0xf\n"
    "v_dot2_f32_bf16 v55, v121, v35, v55\n"
    "v_dot2_f32_bf16 v45, v122, v40, v45\n"
    "v_add_f32_dpp v54, v54, v54 row_half_mirror row_mask:0xf bank_mask:0xf\n"
    "v_dot2_f32_bf16 v55, v122, v36, v55\n"
    "v_dot2_f32_bf16 v45, v123, v41, v45\n"
    "v_add_f32_dpp v54, v54, v54 quad_perm:[2,3,0,1] row_mask:0xf bank_mask:0xf\n"
    "v_dot2_f32_bf16 v55, v123, v37, v55\n"
    "s_waitcnt vmcnt(8)\n"
    "s_cbranch_vccz .Lfd_qa44\n"
    "s_waitcnt vmcnt(5)\n"
    ".Lfd_qa44:\n"
    "global_load_dwordx4 a[28:31], v6, s[8:9] offset:0 nt\n"
    "v_add_u32 v6, s50, v6\n"
    "v_dot2_f32_bf16 v44, v124, v38, 0\n"
    "v_add_f32_dpp v54, v54, v54 quad_perm:[1,0,3,2] row_mask:0xf bank_mask:0xf\n"
    "v_dot2_f32_bf16 v56, v124, v34, 0\n"
    "v_dot2_f32_bf16 v44, v125, v39, v44\n"
    "v_add_f32_dpp v55, v45, v55 row_ror:8 row_mask:0xf bank_mask:0xf\n"
    "v_dot2_f32_bf16 v56, v125, v35, v56\n"
    "v_dot2_f32_bf16 v44, v126, v40, v44\n"
    "v_add_f32_dpp v55, v55, v55 row_half_mirror row_mask:0xf bank_mask:0xf\n"
    "v_dot2_f32_bf16 v56, v126, v36, v56\n"
    "v_dot2_f32_bf16 v44, v127, v41, v44\n"
    "v_add_f32_dpp v55, v55, v55 quad_perm:[2,3,0,1] row_mask:0xf bank_mask:0xf\n"
    "v_dot2_f32_bf16 v56, v127, v37, v56\n"
    "v_dot2_f32_bf16 v45, v128, v38, 0\n"
    "v_add_f32_dpp v55, v55, v55 quad_perm:[1,0,3,2] row_mask:0xf bank_mask:0xf\n"
    "v_dot2_f32_bf16 v57, v128, v34, 0\n"
    "v_dot2_f32_bf16 v45, v129, v39, v45\n"
    "v_add_f32_dpp v56, v44, v56 row_ror:8 row_mask:0xf bank_mask:0xf\n"
    "v_dot2_f32_bf16 v57, v129, v35, v57\n"
    "v_dot2_f32_bf16 v45, v130, v40, v45\n"
    "v_add_f32_dpp v56, v56, v56 row_half_mirror row_mask:0xf bank_mask:0xf\n"
    "v_dot2_f32_bf16 v57, v130, v36, v57\n"
    "v_dot2_f32_bf16 v45, v131, v41, v45\n"
    "v_add_f32_dpp v56, v56, v56 quad_perm:[2,3,0,1] row_mask:0xf bank_mask:0xf\n"
    "v_dot2_f32_bf16 v57, v131, v37, v57\n"
    "v_dot2_f32_bf16 v44, v212, v38, 0\n"
    "v_add_f32_dpp v56, v56, v56 quad_perm:[1,0,3,2] row_mask:0xf bank_mask:0xf\n"
    "v_dot2_f32_bf16 v78, v212, v34, 0\n"
    "v_dot2_f32_bf16 v44, v213, v39, v44\n"
    "v_add_f32_dpp v57, v45, v57 row_ror:8 row_mask:0xf bank_mask:0xf\n"
    "v_dot2_f32_bf16 v78, v213, v35, v78\n"
    "v_dot2_f32_bf16 v44, v214, v40, v44\n"
    "v_add_f32_dpp v57, v57, v57 row_half_mirror row_mask:0xf bank_mask:0xf\n"
    "v_dot2_f32_bf16 v78, v214, v36, v78\n"
    "v_dot2_f32_bf16 v44, v215, v41, v44\n"
    "v_add_f32_dpp v57, v57, v57 quad_perm:[2,3,0,1] row_mask:0xf bank_mask:0xf\n"
    "v_dot2_f32_bf16 v78, v215, v37, v78\n"
    "s_waitcnt vmcnt(8)\n"
    "s_cbranch_vccz .Lfd_qa45\n"
    "s_waitcnt vmcnt(5)\n"
    ".Lfd_qa45:\n"
    "s_mov_b64 exec, s[42:43]\n"
    "global_load_dwordx4 a[112:115], v7, s[8:9] nt\n"
    "s_mov_b64 exec, -1\n"
    "v_dot2_f32_bf16 v45, v132, v38, 0\n"
    "v_add_f32_dpp v57, v57, v57 quad_perm:[1,0,3,2] row_mask:0xf bank_mask:0xf\n"
    "v_dot2_f32_bf16 v58, v132, v34, 0\n"
    "v_dot2_f32_bf16 v45, v133, v39, v45\n"
    "v_add_f32_dpp v78, v44, v78 row_ror:8 row_mask:0xf bank_mask:0xf\n"
    "v_dot2_f32_bf16 v58, v133, v35, v58\n"
    "v_dot2_f32_bf16 v45, v134, v40, v45\n"
    "v_add_f32_dpp v78, v78, v78 row_half_mirror row_mask:0xf bank_mask:0xf\n"
    "v_dot2_f32_bf16 v58, v134, v36, v58\n"
    "v_dot2_f32_bf16 v45, v135, v41, v45\n"
    "v_add_f32_dpp v78, v78, v78 quad_perm:[2,3,0,1] row_mask:0xf bank_mask:0xf\n"
    "v_dot2_f32_bf16 v58, v135, v37, v58\n"
    "v_dot2_f32_bf16 v44, v136, v38, 0\n"
    "v_add_f32_dpp v78, v78, v78 quad_perm:[1,0,3,2] row_mask:0xf bank_mask:0xf\n"
    "v_dot2_f32_bf16 v59, v136, v34, 0\n"
    "v_dot2_f32_bf16 v44, v137, v39, v44\n"
    "v_add_f32_dpp v58, v45, v58 row_ror:8 row_mask:0xf bank_mask:0xf\n"
    "v_dot2_f32_bf16 v59, v137, v35, v59\n"
    "v_dot2_f32_bf16 v44, v138, v40, v44\n"
    "v_add_f32_dpp v58, v58, v58 row_half_mirror row_mask:0xf bank_mask:0xf\n"
    "v_dot2_f32_bf16 v59, v138, v36, v59\n"
    "v_dot2_f32_bf16 v44, v139, v41, v44\n"
    "v_add_f32_dpp v58, v58, v58 quad_perm:[2,3,0,1] row_mask:0xf bank_mask:0xf\n"
    "v_dot2_f32_bf16 v59, v139, v37, v59\n"
    "v_dot2_f32_bf16 v45, v140, v38, 0\n"
    "v_add_f32_dpp v58, v58, v58 quad_perm:[1,0,3,2] row_mask:0xf bank_mask:0xf\n"
    "v_dot2_f32_bf16 v60, v140, v34, 0\n"
    "v_dot2_f32_bf16 v45, v141, v39, v45\n"
    "v_add_f32_dpp v59, v44, v59 row_ror:8 row_mask:0xf bank_mask:0xf\n"
    "v_dot2_f32_bf16 v60, v141, v35, v60\n"
    "v_dot2_f32_bf16 v45, v142, v40, v45\n"
    "v_add_f32_dpp v59, v59, v59 row_half_mirror row_mask:0xf bank_mask:0xf\n"
    "v_dot2_f32_bf16 v60, v142, v36, v60\n"
    "v_dot2_f32_bf16 v45, v143, v41, v45\n"
    "v_add_f32_dpp v59, v59, v59 quad_perm:[2,3,0,1] row_mask:0xf bank_mask:0xf\n"
    "v_dot2_f32_bf16 v60, v143, v37, v60\n"
    "s_waitcnt vmcnt(8)\n"
    "s_cbranch_vccz .Lfd_qa46\n"
    "s_waitcnt vmcnt(5)\n"
    ".Lfd_qa46:\n"
    "global_load_dwordx4 a[32:35], v6, s[8:9] offset:0 nt\n"
    "v_add_u32 v6, s50, v6\n"
    "v_dot2_f32_bf16 v44, v144, v38, 0\n"
    "v_add_f32_dpp v59, v59, v59 quad_perm:[1,0,3,2] row_mask:0xf bank_mask:0xf\n"
    "v_dot2_f32_bf16 v61, v144, v34, 0\n"
    "v_dot2_f32_bf16 v44, v145, v39, v44\n"
    "v_add_f32_dpp v60, v45, v60 row_ror:8 row_mask:0xf bank_mask:0xf\n"
    "v_dot2_f32_bf16 v61, v145, v35, v61\n"
    "v_dot2_f32_bf16 v44, v146, v40, v44\n"
    "v_add_f32_dpp v60, v60, v60 row_half_mirror row_mask:0xf bank_mask:0xf\n"
    "v_dot2_f32_bf16 v61, v146, v36, v61\n"
    "v_dot2_f32_bf16 v44, v147, v41, v44\n"
    "v_add_f32_dpp v60, v60, v60 quad_perm:[2,3,0,1] row_mask:0xf bank_mask:0xf\n"
    "v_dot2_f32_bf16 v61, v147, v37, v61\n"
    "v_dot2_f32_bf16 v45, v148, v38, 0\n"
    "v_add_f32_dpp v60, v60, v60 quad_perm:[1,0,3,2] row_mask:0xf bank_mask:0xf\n"
    "v_dot2_f32_bf16 v62, v148, v34, 0\n"
    "v_dot2_f32_bf16 v45, v149, v39, v45\n"
    "v_add_f32_dpp v61, v44, v61 row_ror:8 row_mask:0xf bank_mask:0xf\n"
    "v_dot2_f32_bf16 v62, v149, v35, v62\n"
    "v_dot2_f32_bf16 v45, v150, v40, v45\n"
    "v_add_f32_dpp v61, v61, v61 row_half_mirror row_mask:0xf bank_mask:0xf\n"
    "v_dot2_f32_bf16 v62, v150, v36, v62\n"
    "v_dot2_f32_bf16 v45, v151, v41, v45\n"
    "v_add_f32_dpp v61, v61, v61 quad_perm:[2,3,0,1] row_mask:0xf bank_mask:0xf\n"
    "v_dot2_f32_bf16 v62, v151, v37, v62\n"
    "v_dot2_f32_bf16 v44, v152, v38, 0\n"
    "v_add_f32_dpp v61, v61, v61 quad_perm:[1,0,3,2] row_mask:0xf bank_mask:0xf\n"
    "v_dot2_f32_bf16 v63, v152, v34, 0\n"
    "v_dot2_f32_bf16 v44, v153, v39, v44\n"
    "v_add_f32_dpp v62, v45, v62 row_ror:8 row_mask:0xf bank_mask:0xf\n"
    "v_dot2_f32_bf16 v63, v153, v35, v63\n"
    "v_dot2_f32_bf16 v44, v154, v40, v44\n"
    "v_add_f32_dpp v62, v62, v62 row_half_mirror row_mask:0xf bank_mask:0xf\n"
    "v_dot2_f32_bf16 v63, v154, v36, v63\n"
    "v_dot2_f32_bf16 v44, v155, v41, v44\n"
    "v_add_f32_dpp v62, v62, v62 quad_perm:[2,3,0,1] row_mask:0xf bank_mask:0xf\n"
    "v_dot2_f32_bf16 v63, v155, v37, v63\n"
    "s_waitcnt vmcnt(8)\n"
    "s_cbranch_vccz .Lfd_qa47\n"
    "s_waitcnt vmcnt(5)\n"
    ".Lfd_qa47:\n"
    "global_load_dwordx4 a[36:39], v6, s[8:9] offset:0 nt\n"
    "v_add_u32 v6, s50, v6\n"
    "v_dot2_f32_bf16 v45, v156, v38, 0\n"
    "v_add_f32_dpp v62, v62, v62 quad_perm:[1,0,3,2] row_mask:0xf bank_mask:0xf\n"
    "v_dot2_f32_bf16 v64, v156, v34, 0\n"
    "v_dot2_f32_bf16 v45, v157, v39, v45\n"
    "v_add_f32_dpp v63, v44, v63 row_ror:8 row_mask:0xf bank_mask:0xf\n"
    "v_dot2_f32_bf16 v64, v157, v35, v64\n"
    "v_dot2_f32_bf16 v45, v158, v40, v45\n"
    "v_add_f32_dpp v63, v63, v63 row_half_mirror row_mask:0xf bank_mask:0xf\n"
    "v_dot2_f32_bf16 v64, v158, v36, v64\n"
    "v_dot2_f32_bf16 v45, v159, v41, v45\n"
    "v_add_f32_dpp v63, v63, v63 quad_perm:[2,3,0,1] row_mask:0xf bank_mask:0xf\n"
    "v_dot2_f32_bf16 v64, v159, v37, v64\n"
    "v_dot2_f32_bf16 v44, v160, v38, 0\n"
    "v_add_f32_dpp v63, v63, v63 quad_perm:[1,0,3,2] row_mask:0xf bank_mask:0xf\n"
    "v_dot2_f32_bf16 v65, v160, v34, 0\n"
    "v_dot2_f32_bf16 v44, v161, v39, v44\n"
    "v_add_f32_dpp v64, v45, v64 row_ror:8 row_mask:0xf bank_mask:0xf\n"
    "v_dot2_f32_bf16 v65, v161, v35, v65\n"
    "v_dot2_f32_bf16 v44, v162, v40, v44\n"
    "v_add_f32_dpp v64, v64, v64 row_half_mirror row_mask:0xf bank_mask:0xf\n"
    "v_dot2_f32_bf16 v65, v162, v36, v65\n"
    "v_dot2_f32_bf16 v44, v163, v41, v44\n"
    "v_add_f32_dpp v64, v64, v64 quad_perm:[2,3,0,1] row_mask:0xf bank_mask:0xf\n"
    "v_dot2_f32_bf16 v65, v163, v37, v65\n"
    "v_dot2_f32_bf16 v45, v164, v38, 0\n"
    "v_add_f32_dpp v64, v64, v64 quad_perm:[1,0,3,2] row_mask:0xf bank_mask:0xf\n"
    "v_dot2_f32_bf16 v66, v164, v34, 0\n"
    "v_dot2_f32_bf16 v45, v165, v39, v45\n"
    "v_add_f32_dpp v65, v44, v65 row_ror:8 row_mask:0xf bank_mask:0xf\n"
    "v_dot2_f32_bf16 v66, v165, v35, v66\n"
    "v_dot2_f32_bf16 v45, v166, v40, v45\n"
    "v_add_f32_dpp v65, v65, v65 row_half_mirror row_mask:0xf bank_mask:0xf\n"
    "v_dot2_f32_bf16 v66, v166, v36, v66\n"
    "v_dot2_f32_bf16 v45, v167, v41, v45\n"
    "v_add_f32_dpp v65, v65, v65 quad_perm:[2,3,0,1] row_mask:0xf bank_mask:0xf\n"
    "v_dot2_f32_bf16 v66, v167, v37, v66\n"
    "s_memrealtime s[98:99]\n"
    "s_waitcnt vmcnt(8)\n"
    "s_waitcnt lgkmcnt(0)\n"
    "s_sub_u32 s98, s98, s96\n"
    "s_cmpk_lt_u32 s98, 464\n"
    "s_cselect_b64 vcc, -1, 0\n"
    "s_cbranch_vccz .Lfd_qa48\n"
    "s_waitcnt vmcnt(5)\n"
    ".Lfd_qa48:\n"
    "global_load_dwordx4 a[40:43], v6, s[8:9] offset:0 nt\n"
    "v_add_u32 v6, s50, v6\n"
    "v_dot2_f32_bf16 v44, v168, v38, 0\n"
    "v_add_f32_dpp v65, v65, v65 quad_perm:[1,0,3,2] row_mask:0xf bank_mask:0xf\n"
    "v_dot2_f32_bf16 v67, v168, v34, 0\n"
    "v_dot2_f32_bf16 v44, v169, v39, v44\n"
    "v_add_f32_dpp v66, v45, v66 row_ror:8 row_mask:0xf bank_mask:0xf\n"
    "v_dot2_f32_bf16 v67, v169, v35, v67\n"
    "v_dot2_f32_bf16 v44, v170, v40, v44\n"
    "v_add_f32_dpp v66, v66, v66 row_half_mirror row_mask:0xf bank_mask:0xf\n"
    "v_dot2_f32_bf16 v67, v170, v36, v67\n"
    "v_dot2_f32_bf16 v44, v171, v41, v44\n"
    "v_add_f32_dpp v66, v66, v66 quad_perm:[2,3,0,1] row_mask:0xf bank_mask:0xf\n"
    "v_dot2_f32_bf16 v67, v171, v37, v67\n"
    "v_dot2_f32_bf16 v45, v172, v38, 0\n"
    "v_add_f32_dpp v66, v66, v66 quad_perm:[1,0,3,2] row_mask:0xf bank_mask:0xf\n"
    "v_dot2_f32_bf16 v68, v172, v34, 0\n"
    "v_dot2_f32_bf16 v45, v173, v39, v45\n"
    "v_add_f32_dpp v67, v44, v67 row_ror:8 row_mask:0xf bank_mask:0xf\n"
    "v_dot2_f32_bf16 v68, v173, v35, v68\n"
    "v_dot2_f32_bf16 v45, v174, v40, v45\n"
    "v_add_f32_dpp v67, v67, v67 row_half_mirror row_mask:0xf bank_mask:0xf\n"
    "v_dot2_f32_bf16 v68, v174, v36, v68\n"
    "v_dot2_f32_bf16 v45, v175, v41, v45\n"
    "v_add_f32_dpp v67, v67, v67 quad_perm:[2,3,0,1] row_mask:0xf bank_mask:0xf\n"
    "v_dot2_f32_bf16 v68, v175, v37, v68\n"
    "v_dot2_f32_bf16 v44, v176, v38, 0\n"
    "v_add_f32_dpp v67, v67, v67 quad_perm:[1,0,3,2] row_mask:0xf bank_mask:0xf\n"
    "v_dot2_f32_bf16 v69, v176, v34, 0\n"
    "v_dot2_f32_bf16 v44, v177, v39, v44\n"
    "v_add_f32_dpp v68, v45, v68 row_ror:8 row_mask:0xf bank_mask:0xf\n"
    "v_dot2_f32_bf16 v69, v177, v35, v69\n"
    "v_dot2_f32_bf16 v44, v178, v40, v44\n"
    "v_add_f32_dpp v68, v68, v68 row_half_mirror row_mask:0xf bank_mask:0xf\n"
    "v_dot2_f32_bf16 v69, v178, v36, v69\n"
    "v_dot2_f32_bf16 v44, v179, v41, v44\n"
    "v_add_f32_dpp v68, v68, v68 quad_perm:[2,3,0,1] row_mask:0xf bank_mask:0xf\n"
    "v_dot2_f32_bf16 v69, v179, v37, v69\n"
    "s_waitcnt vmcnt(8)\n"
    "s_cbranch_vccz .Lfd_qa49\n"
    "s_waitcnt vmcnt(5)\n"
    ".Lfd_qa49:\n"
    "global_load_dwordx4 a[44:47], v6, s[8:9] offset:0 nt\n"
    "v_add_u32 v6, s50, v6\n"
    "v_dot2_f32_bf16 v45, v180, v38, 0\n"
    "v_add_f32_dpp v68, v68, v68 quad_perm:[1,0,3,2] row_mask:0xf bank_mask:0xf\n"
    "v_dot2_f32_bf16 v70, v180, v34, 0\n"
    "v_dot2_f32_bf16 v45, v181, v39, v45\n"
    "v_add_f32_dpp v69, v44, v69 row_ror:8 row_mask:0xf bank_mask:0xf\n"
    "v_dot2_f32_bf16 v70, v181, v35, v70\n"
    "v_dot2_f32_bf16 v45, v182, v40, v45\n"
    "v_add_f32_dpp v69, v69, v69 row_half_mirror row_mask:0xf bank_mask:0xf\n"
    "v_dot2_f32_bf16 v70, v182, v36, v70\n"
    "v_dot2_f32_bf16 v45, v183, v41, v45\n"
    "v_add_f32_dpp v69, v69, v69 quad_perm:[2,3,0,1] row_mask:0xf bank_mask:0xf\n"
    "v_dot2_f32_bf16 v70, v183, v37, v70\n"
    "v_dot2_f32_bf16 v44, v184, v38, 0\n"
    "v_add_f32_dpp v69, v69, v69 quad_perm:[1,0,3,2] row_mask:0xf bank_mask:0xf\n"
    "v_dot2_f32_bf16 v71, v184, v34, 0\n"
    "v_dot2_f32_bf16 v44, v185, v39, v44\n"
    "v_add_f32_dpp v70, v45, v70 row_ror:8 row_mask:0xf bank_mask:0xf\n"
    "v_dot2_f32_bf16 v71, v185, v35, v71\n"
    "v_dot2_f32_bf16 v44, v186, v40, v44\n"
    "v_add_f32_dpp v70, v70, v70 row_half_mirror row_mask:0xf bank_mask:0xf\n"
    "v_dot2_f32_bf16 v71, v186, v36, v71\n"
    "v_dot2_f32_bf16 v44, v187, v41, v44\n"
    "v_add_f32_dpp v70, v70, v70 quad_perm:[2,3,0,1] row_mask:0xf bank_mask:0xf\n"
    "v_dot2_f32_bf16 v71, v187, v37, v71\n"
    "v_dot2_f32_bf16 v45, v188, v38, 0\n"
    "v_add_f32_dpp v70, v70, v70 quad_perm:[1,0,3,2] row_mask:0xf bank_mask:0xf\n"
    "v_dot2_f32_bf16 v72, v188, v34, 0\n"
    "v_dot2_f32_bf16 v45, v189, v39, v45\n"
    "v_add_f32_dpp v71, v44, v71 row_ror:8 row_mask:0xf bank_mask:0xf\n"
    "v_dot2_f32_bf16 v72, v189, v35, v72\n"
    "v_dot2_f32_bf16 v45, v190, v40, v45\n"
    "v_add_f32_dpp v71, v71, v71 row_half_mirror row_mask:0xf bank_mask:0xf\n"
    "v_dot2_f32_bf16 v72, v190, v36, v72\n"
    "v_dot2_f32_bf16 v45, v191, v41, v45\n"
    "v_add_f32_dpp v71, v71, v71 quad_perm:[2,3,0,1] row_mask:0xf bank_mask:0xf\n"
    "v_dot2_f32_bf16 v72, v191, v37, v72\n"
    "s_waitcnt vmcnt(8)\n"
    "s_cbranch_vccz .Lfd_qa50\n"
    "s_waitcnt vmcnt(5)\n"
    ".Lfd_qa50:\n"
    "global_load_dwordx4 a[48:51], v6, s[8:9] offset:0 nt\n"
    "v_add_u32 v6, s50, v6\n"
    "v_dot2_f32_bf16 v44, v192, v38, 0\n"
    "v_add_f32_dpp v71, v71, v71 quad_perm:[1,0,3,2] row_mask:0xf bank_mask:0xf\n"
    "v_dot2_f32_bf16 v73, v192, v34, 0\n"
    "v_dot2_f32_bf16 v44, v193, v39, v44\n"
    "v_add_f32_dpp v72, v45, v72 row_ror:8 row_mask:0xf bank_mask:0xf\n"
    "v_dot2_f32_bf16 v73, v193, v35, v73\n"
    "v_dot2_f32_bf16 v44, v194, v40, v44\n"
    "v_add_f32_dpp v72, v72, v72 row_half_mirror row_mask:0xf bank_mask:0xf\n"
    "v_dot2_f32_bf16 v73, v194, v36, v73\n"
    "v_dot2_f32_bf16 v44, v195, v41, v44\n"
    "v_add_f32_dpp v72, v72, v72 quad_perm:[2,3,0,1] row_mask:0xf bank_mask:0xf\n"
    "v_dot2_f32_bf16 v73, v195, v37, v73\n"
    "v_dot2_f32_bf16 v45, v196, v38, 0\n"
    "v_add_f32_dpp v72, v72, v72 quad_perm:[1,0,3,2] row_mask:0xf bank_mask:0xf\n"
    "v_dot2_f32_bf16 v74, v196, v34, 0\n"
    "v_dot2_f32_bf16 v45, v197, v39, v45\n"
    "v_add_f32_dpp v73, v44, v73 row_ror:8 row_mask:0xf bank_mask:0xf\n"
    "v_dot2_f32_bf16 v74, v197, v35, v74\n"
    "v_dot2_f32_bf16 v45, v198, v40, v45\n"
    "v_add_f32_dpp v73, v73, v73 row_half_mirror row_mask:0xf bank_mask:0xf\n"
    "v_dot2_f32_bf16 v74, v198, v36, v74\n"
    "v_dot2_f32_bf16 v45, v199, v41, v45\n"
    "v_add_f32_dpp v73, v73, v73 quad_perm:[2,3,0,1] row_mask:0xf bank_mask:0xf\n"
    "v_dot2_f32_bf16 v74, v199, v37, v74\n"
    "v_dot2_f32_bf16 v44, v200, v38, 0\n"
    "v_add_f32_dpp v73, v73, v73 quad_perm:[1,0,3,2] row_mask:0xf bank_mask:0xf\n"
    "v_dot2_f32_bf16 v75, v200, v34, 0\n"
    "v_dot2_f32_bf16 v44, v201, v39, v44\n"
    "v_add_f32_dpp v74, v45, v74 row_ror:8 row_mask:0xf bank_mask:0xf\n"
    "v_dot2_f32_bf16 v75, v201, v35, v75\n"
    "v_dot2_f32_bf16 v44, v202, v40, v44\n"
    "v_add_f32_dpp v74, v74, v74 row_half_mirror row_mask:0xf bank_mask:0xf\n"
    "v_dot2_f32_bf16 v75, v202, v36, v75\n"
    "v_dot2_f32_bf16 v44, v203, v41, v44\n"
    "v_add_f32_dpp v74, v74, v74 quad_perm:[2,3,0,1] row_mask:0xf bank_mask:0xf\n"
    "v_dot2_f32_bf16 v75, v203, v37, v75\n"
    "s_waitcnt vmcnt(8)\n"
    "s_cbranch_vccz .Lfd_qa51\n"
    "s_waitcnt vmcnt(5)\n"
    ".Lfd_qa51:\n"
    "global_load_dwordx4 a[52:55], v6, s[8:9] offset:0 nt\n"
    "v_add_u32 v6, s50, v6\n"
    "v_dot2_f32_bf16 v45, v204, v38, 0\n"
    "v_add_f32_dpp v74, v74, v74 quad_perm:[1,0,3,2] row_mask:0xf bank_mask:0xf\n"
    "v_dot2_f32_bf16 v76, v204, v34, 0\n"
    "v_dot2_f32_bf16 v45, v205, v39, v45\n"
    "v_add_f32_dpp v75, v44, v75 row_ror:8 row_mask:0xf bank_mask:0xf\n"
    "v_dot2_f32_bf16 v76, v205, v35, v76\n"
    "v_dot2_f32_bf16 v45, v206, v40, v45\n"
    "v_add_f32_dpp v75, v75, v75 row_half_mirror row_mask:0xf bank_mask:0xf\n"
    "v_dot2_f32_bf16 v76, v206, v36, v76\n"
    "v_dot2_f32_bf16 v45, v207, v41, v45\n"
    "v_add_f32_dpp v75, v75, v75 quad_perm:[2,3,0,1] row_mask:0xf bank_mask:0xf\n"
    "v_dot2_f32_bf16 v76, v207, v37, v76\n"
    "v_dot2_f32_bf16 v44, v208, v38, 0\n"
    "v_add_f32_dpp v75, v75, v75 quad_perm:[1,0,3,2] row_mask:0xf bank_mask:0xf\n"
    "v_dot2_f32_bf16 v77, v208, v34, 0\n"
    "v_dot2_f32_bf16 v44, v209, v39, v44\n"
    "v_add_f32_dpp v76, v45, v76 row_ror:8 row_mask:0xf bank_mask:0xf\n"
    "v_dot2_f32_bf16 v77, v209, v35, v77\n"
    "v_dot2_f32_bf16 v44, v210, v40, v44\n"
    "v_add_f32_dpp v76, v76, v76 row_half_mirror row_mask:0xf bank_mask:0xf\n"
    "v_dot2_f32_bf16 v77, v210, v36, v77\n"
    "v_dot2_f32_bf16 v44, v211, v41, v44\n"
    "v_add_f32_dpp v76, v76, v76 quad_perm:[2,3,0,1] row_mask:0xf bank_mask:0xf\n"
    "v_dot2_f32_bf16 v77, v211, v37, v77\n"
    "s_nop 0\n"
    "v_add_f32_dpp v76, v76, v76 quad_perm:[1,0,3,2] row_mask:0xf bank_mask:0xf\n"
    "v_mov_b32 v79, 0xff800000\n"
    "v_cndmask_b32 v78, v79, v78, s[42:43]\n"
    "v_add_f32_dpp v77, v44, v77 row_ror:8 row_mask:0xf bank_mask:0xf\n"
    "s_nop 1\n"
    "v_add_f32_dpp v77, v77, v77 row_half_mirror row_mask:0xf bank_mask:0xf\n"
    "s_nop 1\n"
    "v_add_f32_dpp v77, v77, v77 quad_perm:[2,3,0,1] row_mask:0xf bank_mask:0xf\n"
    "s_nop 1\n"
    "v_add_f32_dpp v77, v77, v77 quad_perm:[1,0,3,2] row_mask:0xf bank_mask:0xf\n"
    "s_waitcnt vmcnt(8)\n"
    "s_cbranch_vccz .Lfd_qa52\n"
    "s_waitcnt vmcnt(5)\n"
    ".Lfd_qa52:\n"
    "global_load_dwordx4 a[56:59], v6, s[8:9] offset:0 nt\n"
    "v_add_u32 v6, s50, v6\n"
    "v_cvt_pk_bf16_f32 v51, v50, v51\n"
    "v_lshlrev_b32 v50, 16, v51\n"
    "v_and_b32 v51, 0xffff0000, v51\n"
    "v_mul_f32 v50, 0x3db504f3, v50\n"
    "v_mul_f32 v51, 0x3db504f3, v51\n"
    "v_cvt_pk_bf16_f32 v51, v50, v51\n"
    "v_lshlrev_b32 v50, 16, v51\n"
    "v_and_b32 v51, 0xffff0000, v51\n"
    "v_max_f32 v42, v50, v51\n"
    "v_cvt_pk_bf16_f32 v53, v52, v53\n"
    "v_lshlrev_b32 v52, 16, v53\n"
    "v_and_b32 v53, 0xffff0000, v53\n"
    "v_mul_f32 v52, 0x3db504f3, v52\n"
    "v_mul_f32 v53, 0x3db504f3, v53\n"
    "v_cvt_pk_bf16_f32 v53, v52, v53\n"
    "v_lshlrev_b32 v52, 16, v53\n"
    "v_and_b32 v53, 0xffff0000, v53\n"
    "v_max3_f32 v42, v42, v52, v53\n"
    "v_cvt_pk_bf16_f32 v55, v54, v55\n"
    "v_lshlrev_b32 v54, 16, v55\n"
    "v_and_b32 v55, 0xffff0000, v55\n"
    "v_mul_f32 v54, 0x3db504f3, v54\n"
    "v_mul_f32 v55, 0x3db504f3, v55\n"
    "v_cvt_pk_bf16_f32 v55, v54, v55\n"
    "v_lshlrev_b32 v54, 16, v55\n"
    "v_and_b32 v55, 0xffff0000, v55\n"
    "v_max3_f32 v42, v42, v54, v55\n"
    "v_cvt_pk_bf16_f32 v57, v56, v57\n"
    "v_lshlrev_b32 v56, 16, v57\n"
    "v_and_b32 v57, 0xffff0000, v57\n"
    "v_mul_f32 v56, 0x3db504f3, v56\n"
    "v_mul_f32 v57, 0x3db504f3, v57\n"
    "v_cvt_pk_bf16_f32 v57, v56, v57\n"
    "v_lshlrev_b32 v56, 16, v57\n"
    "v_and_b32 v57, 0xffff0000, v57\n"
    "v_max3_f32 v42, v42, v56, v57\n"
    "v_cvt_pk_bf16_f32 v59, v58, v59\n"
    "v_lshlrev_b32 v58, 16, v59\n"
    "v_and_b32 v59, 0xffff0000, v59\n"
    "v_mul_f32 v58, 0x3db504f3, v58\n"
    "v_mul_f32 v59, 0x3db504f3, v59\n"
    "v_cvt_pk_bf16_f32 v59, v58, v59\n"
    "v_lshlrev_b32 v58, 16, v59\n"
    "v_and_b32 v59, 0xffff0000, v59\n"
    "v_max3_f32 v42, v42, v58, v59\n"
    "s_memrealtime s[98:99]\n"
    "s_waitcnt vmcnt(8)\n"
    "s_waitcnt lgkmcnt(0)\n"
    "s_sub_u32 s98, s98, s96\n"
    "s_cmpk_lt_u32 s98, 515\n"
    "s_cselect_b64 vcc, -1, 0\n"
    "s_cbranch_vccz .Lfd_qa53\n"
    "s_waitcnt vmcnt(5)\n"
    ".Lfd_qa53:\n"
    "global_load_dwordx4 a[60:63], v6, s[8:9] offset:0 nt\n"
    "v_add_u32 v6, s50, v6\n"
    "v_cvt_pk_bf16_f32 v61, v60, v61\n"
    "v_lshlrev_b32 v60, 16, v61\n"
    "v_and_b32 v61, 0xffff0000, v61\n"
    "v_mul_f32 v60, 0x3db504f3, v60\n"
    "v_mul_f32 v61, 0x3db504f3, v61\n"
    "v_cvt_pk_bf16_f32 v61, v60, v61\n"
    "v_lshlrev_b32 v60, 16, v61\n"
    "v_and_b32 v61, 0xffff0000, v61\n"
    "v_max3_f32 v42, v42, v60, v61\n"
    "v_cvt_pk_bf16_f32 v63, v62, v63\n"
    "v_lshlrev_b32 v62, 16, v63\n"
    "v_and_b32 v63, 0xffff0000, v63\n"
    "v_mul_f32 v62, 0x3db504f3, v62\n"
    "v_mul_f32 v63, 0x3db504f3, v63\n"
    "v_cvt_pk_bf16_f32 v63, v62, v63\n"
    "v_lshlrev_b32 v62, 16, v63\n"
    "v_and_b32 v63, 0xffff0000, v63\n"
    "v_max3_f32 v42, v42, v62, v63\n"
    "v_cvt_pk_bf16_f32 v65, v64, v65\n"
    "v_lshlrev_b32 v64, 16, v65\n"
    "v_and_b32 v65, 0xffff0000, v65\n"
    "v_mul_f32 v64, 0x3db504f3, v64\n"
    "v_mul_f32 v65, 0x3db504f3, v65\n"
    "v_cvt_pk_bf16_f32 v65, v64, v65\n"
    "v_lshlrev_b32 v64, 16, v65\n"
    "v_and_b32 v65, 0xffff0000, v65\n"
    "v_max3_f32 v42, v42, v64, v65\n"
    "v_cvt_pk_bf16_f32 v67, v66, v67\n"
    "v_lshlrev_b32 v66, 16, v67\n"
    "v_and_b32 v67, 0xffff0000, v67\n"
    "v_mul_f32 v66, 0x3db504f3, v66\n"
    "v_mul_f32 v67, 0x3db504f3, v67\n"
    "v_cvt_pk_bf16_f32 v67, v66, v67\n"
    "v_lshlrev_b32 v66, 16, v67\n"
    "v_and_b32 v67, 0xffff0000, v67\n"
    "v_max3_f32 v42, v42, v66, v67\n"
    "v_cvt_pk_bf16_f32 v69, v68, v69\n"
    "v_lshlrev_b32 v68, 16, v69\n"
    "v_and_b32 v69, 0xffff0000, v69\n"
    "v_mul_f32 v68, 0x3db504f3, v68\n"
    "v_mul_f32 v69, 0x3db504f3, v69\n"
    "v_cvt_pk_bf16_f32 v69, v68, v69\n"
    "v_lshlrev_b32 v68, 16, v69\n"
    "v_and_b32 v69, 0xffff0000, v69\n"
    "v_max3_f32 v42, v42, v68, v69\n"
    "s_waitcnt vmcnt(8)\n"
    "s_cbranch_vccz .Lfd_qa54\n"
    "s_waitcnt vmcnt(5)\n"
    ".Lfd_qa54:\n"
    "global_load_dwordx4 a[64:67], v6, s[8:9] offset:0 nt\n"
    "v_add_u32 v6, s50, v6\n"
    "v_cvt_pk_bf16_f32 v71, v70, v71\n"
    "v_lshlrev_b32 v70, 16, v71\n"
    "v_and_b32 v71, 0xffff0000, v71\n"
    "v_mul_f32 v70, 0x3db504f3, v70\n"
    "v_mul_f32 v71, 0x3db504f3, v71\n"
    "v_cvt_pk_bf16_f32 v71, v70, v71\n"
    "v_lshlrev_b32 v70, 16, v71\n"
    "v_and_b32 v71, 0xffff0000, v71\n"
    "v_max3_f32 v42, v42, v70, v71\n"
    "v_cvt_pk_bf16_f32 v73, v72, v73\n"
    "v_lshlrev_b32 v72, 16, v73\n"
    "v_and_b32 v73, 0xffff0000, v73\n"
    "v_mul_f32 v72, 0x3db504f3, v72\n"
    "v_mul_f32 v73, 0x3db504f3, v73\n"
    "v_cvt_pk_bf16_f32 v73, v72, v73\n"
    "v_lshlrev_b32 v72, 16, v73\n"
    "v_and_b32 v73, 0xffff0000, v73\n"
    "v_max3_f32 v42, v42, v72, v73\n"
    "v_cvt_pk_bf16_f32 v75, v74, v75\n"
    "v_lshlrev_b32 v74, 16, v75\n"
    "v_and_b32 v75, 0xffff0000, v75\n"
    "v_mul_f32 v74, 0x3db504f3, v74\n"
    "v_mul_f32 v75, 0x3db504f3, v75\n"
    "v_cvt_pk_bf16_f32 v75, v74, v75\n"
    "v_lshlrev_b32 v74, 16, v75\n"
    "v_and_b32 v75, 0xffff0000, v75\n"
    "v_max3_f32 v42, v42, v74, v75\n"
    "v_cvt_pk_bf16_f32 v77, v76, v77\n"
    "v_lshlrev_b32 v76, 16, v77\n"
    "v_and_b32 v77, 0xffff0000, v77\n"
    "v_mul_f32 v76, 0x3db504f3, v76\n"
    "v_mul_f32 v77, 0x3db504f3, v77\n"
    "v_cvt_pk_bf16_f32 v77, v76, v77\n"
    "v_lshlrev_b32 v76, 16, v77\n"
    "v_and_b32 v77, 0xffff0000, v77\n"
    "v_max3_f32 v42, v42, v76, v77\n"
    "v_cvt_pk_bf16_f32 v79, v78, v79\n"
    "v_lshlrev_b32 v78, 16, v79\n"
    "v_and_b32 v79, 0xffff0000, v79\n"
    "v_mul_f32 v78, 0x3db504f3, v78\n"
    "v_mul_f32 v79, 0x3db504f3, v79\n"
    "v_cvt_pk_bf16_f32 v79, v78, v79\n"
    "v_lshlrev_b32 v78, 16, v79\n"
    "v_and_b32 v79, 0xffff0000, v79\n"
    "v_max3_f32 v42, v42, v78, v79\n"
    "s_waitcnt vmcnt(8)\n"
    "s_cbranch_vccz .Lfd_qa55\n"
    "s_waitcnt vmcnt(5)\n"
    ".Lfd_qa55:\n"
    "global_load_dwordx4 a[68:71], v6, s[8:9] offset:0 nt\n"
    "v_add_u32 v6, s50, v6\n"
    "v_mov_b32 v44, v42\n"
    "s_nop 1\n"
    "v_permlane16_swap_b32 v42, v44\n"
    "v_max_f32 v42, v42, v44\n"
    "v_mov_b32 v44, v42\n"
    "s_nop 1\n"
    "v_permlane32_swap_b32 v42, v44\n"
    "v_max_f32 v42, v42, v44\n"
    "s_mov_b32 s34, 0x3fb8aa3b\n"
    "v_mul_f32 v44, s34, v42\n"
    "v_fma_f32 v50, v50, s34, -v44\n"
    "v_fma_f32 v51, v51, s34, -v44\n"
    "v_exp_f32 v50, v50\n"
    "v_exp_f32 v51, v51\n"
    "v_fma_f32 v52, v52, s34, -v44\n"
    "v_fma_f32 v53, v53, s34, -v44\n"
    "v_exp_f32 v52, v52\n"
    "v_exp_f32 v53, v53\n"
    "v_cvt_pk_bf16_f32 v80, v50, v51\n"
    "v_mov_b64 v[46:47], v[50:51]\n"
    "v_fma_f32 v54, v54, s34, -v44\n"
    "v_fma_f32 v55, v55, s34, -v44\n"
    "v_exp_f32 v54, v54\n"
    "v_exp_f32 v55, v55\n"
    "v_cvt_pk_bf16_f32 v81, v52, v53\n"
    "v_pk_add_f32 v[46:47], v[46:47], v[52:53]\n"
    "v_fma_f32 v56, v56, s34, -v44\n"
    "v_fma_f32 v57, v57, s34, -v44\n"
    "v_exp_f32 v56, v56\n"
    "v_exp_f32 v57, v57\n"
    "v_cvt_pk_bf16_f32 v82, v54, v55\n"
    "v_pk_add_f32 v[46:47], v[46:47], v[54:55]\n"
    "v_fma_f32 v58, v58, s34, -v44\n"
    "v_fma_f32 v59, v59, s34, -v44\n"
    "v_exp_f32 v58, v58\n"
    "v_exp_f32 v59, v59\n"
    "v_cvt_pk_bf16_f32 v83, v56, v57\n"
    "v_pk_add_f32 v[46:47], v[46:47], v[56:57]\n"
    "s_waitcnt vmcnt(8)\n"
    "s_cbranch_vccz .Lfd_qa56\n"
    "s_waitcnt vmcnt(5)\n"
    ".Lfd_qa56:\n"
    "global_load_dwordx4 a[72:75], v6, s[8:9] offset:0 nt\n"
    "v_add_u32 v6, s50, v6\n"
    "v_fma_f32 v60, v60, s34, -v44\n"
    "v_fma_f32 v61, v61, s34, -v44\n"
    "v_exp_f32 v60, v60\n"
    "v_exp_f32 v61, v61\n"
    "v_cvt_pk_bf16_f32 v84, v58, v59\n"
    "v_pk_add_f32 v[46:47], v[46:47], v[58:59]\n"
    "v_fma_f32 v62, v62, s34, -v44\n"
    "v_fma_f32 v63, v63, s34, -v44\n"
    "v_exp_f32 v62, v62\n"
    "v_exp_f32 v63, v63\n"
    "v_cvt_pk_bf16_f32 v85, v60, v61\n"
    "v_pk_add_f32 v[46:47], v[46:47], v[60:61]\n"
    "v_fma_f32 v64, v64, s34, -v44\n"
    "v_fma_f32 v65, v65, s34, -v44\n"
    "v_exp_f32 v64, v64\n"
    "v_exp_f32 v65, v65\n"
    "v_cvt_pk_bf16_f32 v86, v62, v63\n"
    "v_pk_add_f32 v[46:47], v[46:47], v[62:63]\n"
    "v_fma_f32 v66, v66, s34, -v44\n"
    "v_fma_f32 v67, v67, s34, -v44\n"
    "v_exp_f32 v66, v66\n"
    "v_exp_f32 v67, v67\n"
    "v_cvt_pk_bf16_f32 v87, v64, v65\n"
    "v_pk_add_f32 v[46:47], v[46:47], v[64:65]\n"
    "v_fma_f32 v68, v68, s34, -v44\n"
    "v_fma_f32 v69, v69, s34, -v44\n"
    "v_exp_f32 v68, v68\n"
    "v_exp_f32 v69, v69\n"
    "v_cvt_pk_bf16_f32 v88, v66, v67\n"
    "v_pk_add_f32 v[46:47], v[46:47], v[66:67]\n"
    "s_waitcnt vmcnt(8)\n"
    "s_cbranch_vccz .Lfd_qa57\n"
    "s_waitcnt vmcnt(5)\n"
    ".Lfd_qa57:\n"
    "global_load_dwordx4 a[76:79], v6, s[8:9] offset:0 nt\n"
    "v_add_u32 v6, s50, v6\n"
    "v_fma_f32 v70, v70, s34, -v44\n"
    "v_fma_f32 v71, v71, s34, -v44\n"
    "v_exp_f32 v70, v70\n"
    "v_exp_f32 v71, v71\n"
    "v_cvt_pk_bf16_f32 v89, v68, v69\n"
    "v_pk_add_f32 v[46:47], v[46:47], v[68:69]\n"
    "v_fma_f32 v72, v72, s34, -v44\n"
    "v_fma_f32 v73, v73, s34, -v44\n"
    "v_exp_f32 v72, v72\n"
    "v_exp_f32 v73, v73\n"
    "v_cvt_pk_bf16_f32 v90, v70, v71\n"
    "v_pk_add_f32 v[46:47], v[46:47], v[70:71]\n"
    "v_fma_f32 v74, v74, s34, -v44\n"
    "v_fma_f32 v75, v75, s34, -v44\n"
    "v_exp_f32 v74, v74\n"
    "v_exp_f32 v75, v75\n"
    "v_cvt_pk_bf16_f32 v91, v72, v73\n"
    "v_pk_add_f32 v[46:47], v[46:47], v[72:73]\n"
    "v_fma_f32 v76, v76, s34, -v44\n"
    "v_fma_f32 v77, v77, s34, -v44\n"
    "v_exp_f32 v76, v76\n"
    "v_exp_f32 v77, v77\n"
    "v_cvt_pk_bf16_f32 v92, v74, v75\n"
    "v_pk_add_f32 v[46:47], v[46:47], v[74:75]\n"
    "v_fma_f32 v78, v78, s34, -v44\n"
    "v_fma_f32 v79, v79, s34, -v44\n"
    "v_exp_f32 v78, v78\n"
    "v_exp_f32 v79, v79\n"
    "v_cvt_pk_bf16_f32 v93, v76, v77\n"
    "v_pk_add_f32 v[46:47], v[46:47], v[76:77]\n"
    "s_memrealtime s[98:99]\n"
    "s_waitcnt vmcnt(8)\n"
    "s_waitcnt lgkmcnt(0)\n"
    "s_sub_u32 s98, s98, s96\n"
    "s_cmpk_lt_u32 s98, 567\n"
    "s_cselect_b64 vcc, -1, 0\n"
    "s_cbranch_vccz .Lfd_qa58\n"
    "s_waitcnt vmcnt(5)\n"
    ".Lfd_qa58:\n"
    "global_load_dwordx4 a[80:83], v6, s[8:9] offset:0 nt\n"
    "v_add_u32 v6, s50, v6\n"
    "v_cvt_pk_bf16_f32 v94, v78, v79\n"
    "v_pk_add_f32 v[46:47], v[46:47], v[78:79]\n"
    "v_add_f32 v43, v46, v47\n"
    "v_mov_b32 v44, v43\n"
    "s_nop 1\n"
    "v_permlane16_swap_b32 v43, v44\n"
    "v_add_f32 v43, v43, v44\n"
    "v_mov_b32 v44, v43\n"
    "s_nop 1\n"
    "v_permlane32_swap_b32 v43, v44\n"
    "v_add_f32 v43, v43, v44\n"
    "s_lshl_b32 s34, s3, 3\n"
    "v_lshl_add_u32 v44, v0, 2, s34\n"
    "s_mov_b64 exec, 0x101\n"
    "ds_write_b64 v44, v[42:43] offset:1856\n"
    "s_mov_b64 exec, -1\n"
    "s_lshl_b32 s34, s3, 10\n"
    "v_lshrrev_b32 v44, 5, v0\n"
    "v_lshl_add_u32 v44, v44, 9, s34\n"
    "v_and_b32 v45, 15, v0\n"
    "v_lshl_add_u32 v44, v45, 5, v44\n"
    "v_and_b32 v45, 16, v0\n"
    "v_add_u32 v44, v44, v45\n"
    "v_lshlrev_b32 v45, 4, v1\n"
    "s_waitcnt lgkmcnt(0)\n"
    "s_barrier\n"
    "v_perm_b32 v22, v80, v80, v10\n"
    "v_perm_b32 v23, v80, v80, v11\n"
    "v_perm_b32 v24, v80, v80, v12\n"
    "v_perm_b32 v25, v80, v80, v13\n"
    "s_nop 1\n"
    "v_mfma_f32_16x16x32_bf16 v[30:33], v[22:25], a[0:3], 0\n"
    "s_waitcnt vmcnt(8)\n"
    "s_cbranch_vccz .Lfd_qa59\n"
    "s_waitcnt vmcnt(5)\n"
    ".Lfd_qa59:\n"
    "global_load_dwordx4 a[84:87], v6, s[8:9] offset:0 nt\n"
    "v_add_u32 v6, s50, v6\n"
    "v_perm_b32 v26, v80, v80, v14\n"
    "v_perm_b32 v27, v80, v80, v15\n"
    "v_perm_b32 v28, v80, v80, v16\n"
    "v_perm_b32 v29, v80, v80, v17\n"
    "s_nop 1\n"
    "v_mfma_f32_16x16x32_bf16 v[30:33], v[26:29], a[4:7], v[30:33]\n"
    "v_perm_b32 v22, v81, v81, v10\n"
    "v_perm_b32 v23, v81, v81, v11\n"
    "v_perm_b32 v24, v81, v81, v12\n"
    "v_perm_b32 v25, v81, v81, v13\n"
    "s_nop 1\n"
    "v_mfma_f32_16x16x32_bf16 v[30:33], v[22:25], a[8:11], v[30:33]\n"
    "v_perm_b32 v26, v81, v81, v14\n"
    "v_perm_b32 v27, v81, v81, v15\n"
    "v_perm_b32 v28, v81, v81, v16\n"
    "v_perm_b32 v29, v81, v81, v17\n"
    "s_nop 1\n"
    "v_mfma_f32_16x16x32_bf16 v[30:33], v[26:29], a[12:15], v[30:33]\n"
    "s_waitcnt vmcnt(8)\n"
    "s_cbranch_vccz .Lfd_qa60\n"
    "s_waitcnt vmcnt(5)\n"
    ".Lfd_qa60:\n"
    "global_load_dwordx4 a[88:91], v6, s[8:9] offset:0 nt\n"
    "v_add_u32 v6, s50, v6\n"
    "v_perm_b32 v22, v82, v82, v10\n"
    "v_perm_b32 v23, v82, v82, v11\n"
    "v_perm_b32 v24, v82, v82, v12\n"
    "v_perm_b32 v25, v82, v82, v13\n"
    "s_nop 1\n"
    "v_mfma_f32_16x16x32_bf16 v[30:33], v[22:25], a[16:19], v[30:33]\n"
    "v_perm_b32 v26, v82, v82, v14\n"
    "v_perm_b32 v27, v82, v82, v15\n"
    "v_perm_b32 v28, v82, v82, v16\n"
    "v_perm_b32 v29, v82, v82, v17\n"
    "s_nop 1\n"
    "v_mfma_f32_16x16x32_bf16 v[30:33], v[26:29], a[20:23], v[30:33]\n"
    "v_perm_b32 v22, v83, v83, v10\n"
    "v_perm_b32 v23, v83, v83, v11\n"
    "v_perm_b32 v24, v83, v83, v12\n"
    "v_perm_b32 v25, v83, v83, v13\n"
    "s_nop 1\n"
    "v_mfma_f32_16x16x32_bf16 v[30:33], v[22:25], a[24:27], v[30:33]\n"
    "s_waitcnt vmcnt(8)\n"
    "s_cbranch_vccz .Lfd_qa61\n"
    "s_waitcnt vmcnt(5)\n"
    ".Lfd_qa61:\n"
    "global_load_dwordx4 a[92:95], v6, s[8:9] offset:0 nt\n"
    "v_add_u32 v6, s50, v6\n"
    "v_perm_b32 v26, v83, v83, v14\n"
    "v_perm_b32 v27, v83, v83, v15\n"
    "v_perm_b32 v28, v83, v83, v16\n"
    "v_perm_b32 v29, v83, v83, v17\n"
    "s_nop 1\n"
    "v_mfma_f32_16x16x32_bf16 v[30:33], v[26:29], a[28:31], v[30:33]\n"
    "v_perm_b32 v22, v84, v84, v10\n"
    "v_perm_b32 v23, v84, v84, v11\n"
    "v_perm_b32 v24, v84, v84, v12\n"
    "v_perm_b32 v25, v84, v84, v13\n"
    "s_nop 1\n"
    "v_mfma_f32_16x16x32_bf16 v[30:33], v[22:25], a[32:35], v[30:33]\n"
    "v_perm_b32 v26, v84, v84, v14\n"
    "v_perm_b32 v27, v84, v84, v15\n"
    "v_perm_b32 v28, v84, v84, v16\n"
    "v_perm_b32 v29, v84, v84, v17\n"
    "s_nop 1\n"
    "v_mfma_f32_16x16x32_bf16 v[30:33], v[26:29], a[36:39], v[30:33]\n"
    "s_waitcnt vmcnt(8)\n"
    "s_cbranch_vccz .Lfd_qa62\n"
    "s_waitcnt vmcnt(5)\n"
    ".Lfd_qa62:\n"
    "global_load_dwordx4 a[96:99], v6, s[8:9] offset:0 nt\n"
    "v_add_u32 v6, s50, v6\n"
    "v_perm_b32 v22, v94, v94, v10\n"
    "v_perm_b32 v23, v94, v94, v11\n"
    "v_perm_b32 v24, v94, v94, v12\n"
    "v_perm_b32 v25, v94, v94, v13\n"
    "s_nop 1\n"
    "v_mfma_f32_16x16x32_bf16 v[30:33], v[22:25], a[112:115], v[30:33]\n"
    "v_perm_b32 v22, v85, v85, v10\n"
    "v_perm_b32 v23, v85, v85, v11\n"
    "v_perm_b32 v24, v85, v85, v12\n"
    "v_perm_b32 v25, v85, v85, v13\n"
    "s_nop 1\n"
    "v_mfma_f32_16x16x32_bf16 v[30:33], v[22:25], a[40:43], v[30:33]\n"
    "v_perm_b32 v26, v85, v85, v14\n"
    "v_perm_b32 v27, v85, v85, v15\n"
    "v_perm_b32 v28, v85, v85, v16\n"
    "v_perm_b32 v29, v85, v85, v17\n"
    "s_nop 1\n"
    "v_mfma_f32_16x16x32_bf16 v[30:33], v[26:29], a[44:47], v[30:33]\n"
    "v_perm_b32 v22, v86, v86, v10\n"
    "v_perm_b32 v23, v86, v86, v11\n"
    "v_perm_b32 v24, v86, v86, v12\n"
    "v_perm_b32 v25, v86, v86, v13\n"
    "s_nop 1\n"
    "v_mfma_f32_16x16x32_bf16 v[30:33], v[22:25], a[48:51], v[30:33]\n"
    "s_memrealtime s[98:99]\n"
    "s_waitcnt vmcnt(8)\n"
    "s_waitcnt lgkmcnt(0)\n"
    "s_sub_u32 s98, s98, s96\n"
    "s_cmpk_lt_u32 s98, 642\n"
    "s_cselect_b64 vcc, -1, 0\n"
    "s_cbranch_vccz .Lfd_qa63\n"
    "s_waitcnt vmcnt(5)\n"
    ".Lfd_qa63:\n"
    "global_load_dwordx4 a[100:103], v6, s[8:9] offset:0 nt\n"
    "v_add_u32 v6, s50, v6\n"
    "v_perm_b32 v26, v86, v86, v14\n"
    "v_perm_b32 v27, v86, v86, v15\n"
    "v_perm_b32 v28, v86, v86, v16\n"
    "v_perm_b32 v29, v86, v86, v17\n"
    "s_nop 1\n"
    "v_mfma_f32_16x16x32_bf16 v[30:33], v[26:29], a[52:55], v[30:33]\n"
    "v_perm_b32 v22, v87, v87, v10\n"
    "v_perm_b32 v23, v87, v87, v11\n"
    "v_perm_b32 v24, v87, v87, v12\n"
    "v_perm_b32 v25, v87, v87, v13\n"
    "s_nop 1\n"
    "v_mfma_f32_16x16x32_bf16 v[30:33], v[22:25], a[56:59], v[30:33]\n"
    "v_perm_b32 v26, v87, v87, v14\n"
    "v_perm_b32 v27, v87, v87, v15\n"
    "v_perm_b32 v28, v87, v87, v16\n"
    "v_perm_b32 v29, v87, v87, v17\n"
    "s_nop 1\n"
    "v_mfma_f32_16x16x32_bf16 v[30:33], v[26:29], a[60:63], v[30:33]\n"
    "s_waitcnt vmcnt(8)\n"
    "s_cbranch_vccz .Lfd_qa64\n"
    "s_waitcnt vmcnt(5)\n"
    ".Lfd_qa64:\n"
    "global_load_dwordx4 a[104:107], v6, s[8:9] offset:0 nt\n"
    "v_add_u32 v6, s50, v6\n"
    "v_perm_b32 v22, v88, v88, v10\n"
    "v_perm_b32 v23, v88, v88, v11\n"
    "v_perm_b32 v24, v88, v88, v12\n"
    "v_perm_b32 v25, v88, v88, v13\n"
    "s_nop 1\n"
    "v_mfma_f32_16x16x32_bf16 v[30:33], v[22:25], a[64:67], v[30:33]\n"
    "v_perm_b32 v26, v88, v88, v14\n"
    "v_perm_b32 v27, v88, v88, v15\n"
    "v_perm_b32 v28, v88, v88, v16\n"
    "v_perm_b32 v29, v88, v88, v17\n"
    "s_nop 1\n"
    "v_mfma_f32_16x16x32_bf16 v[30:33], v[26:29], a[68:71], v[30:33]\n"
    "v_perm_b32 v22, v89, v89, v10\n"
    "v_perm_b32 v23, v89, v89, v11\n"
    "v_perm_b32 v24, v89, v89, v12\n"
    "v_perm_b32 v25, v89, v89, v13\n"
    "s_waitcnt vmcnt(8)\n"
    "s_cbranch_vccz .Lfd_qa65\n"
    "s_waitcnt vmcnt(5)\n"
    ".Lfd_qa65:\n"
    "global_load_dwordx4 a[108:111], v6, s[8:9] offset:0 nt\n"
    "v_mfma_f32_16x16x32_bf16 v[30:33], v[22:25], a[72:75], v[30:33]\n"
    "v_perm_b32 v26, v89, v89, v14\n"
    "v_perm_b32 v27, v89, v89, v15\n"
    "v_perm_b32 v28, v89, v89, v16\n"
    "v_perm_b32 v29, v89, v89, v17\n"
    "s_waitcnt vmcnt(8)\n"
    "s_cbranch_vccz .Lfd_qa66\n"
    "s_waitcnt vmcnt(5)\n"
    ".Lfd_qa66:\n"
    "global_load_dwordx4 a[116:119], v9, s[12:13] offset:-4032 nt\n"
    "v_mfma_f32_16x16x32_bf16 v[30:33], v[26:29], a[76:79], v[30:33]\n"
    "v_perm_b32 v22, v90, v90, v10\n"
    "v_perm_b32 v23, v90, v90, v11\n"
    "v_perm_b32 v24, v90, v90, v12\n"
    "v_perm_b32 v25, v90, v90, v13\n"
    "s_waitcnt vmcnt(8)\n"
    "s_cbranch_vccz .Lfd_qa67\n"
    "s_waitcnt vmcnt(5)\n"
    ".Lfd_qa67:\n"
    "global_load_dwordx4 a[120:123], v9, s[12:13] offset:-2880 nt\n"
    "v_mfma_f32_16x16x32_bf16 v[30:33], v[22:25], a[80:83], v[30:33]\n"
    "v_perm_b32 v26, v90, v90, v14\n"
    "v_perm_b32 v27, v90, v90, v15\n"
    "v_perm_b32 v28, v90, v90, v16\n"
    "v_perm_b32 v29, v90, v90, v17\n"
    "s_memrealtime s[98:99]\n"
    "s_waitcnt vmcnt(8)\n"
    "s_waitcnt lgkmcnt(0)\n"
    "s_sub_u32 s98, s98, s96\n"
    "s_cmpk_lt_u32 s98, 717\n"
    "s_cselect_b64 vcc, -1, 0\n"
    "s_cbranch_vccz .Lfd_qa68\n"
    "s_waitcnt vmcnt(5)\n"
    ".Lfd_qa68:\n"
    "global_load_dwordx4 a[124:127], v9, s[12:13] offset:-1728 nt\n"
    "v_mfma_f32_16x16x32_bf16 v[30:33], v[26:29], a[84:87], v[30:33]\n"
    "v_perm_b32 v22, v91, v91, v10\n"
    "v_perm_b32 v23, v91, v91, v11\n"
    "v_perm_b32 v24, v91, v91, v12\n"
    "v_perm_b32 v25, v91, v91, v13\n"
    "s_waitcnt vmcnt(8)\n"
    "s_cbranch_vccz .Lfd_qa69\n"
    "s_waitcnt vmcnt(5)\n"
    ".Lfd_qa69:\n"
    "global_load_dwordx4 a[128:131], v9, s[12:13] offset:-576 nt\n"
    "v_mfma_f32_16x16x32_bf16 v[30:33], v[22:25], a[88:91], v[30:33]\n"
    "v_perm_b32 v26, v91, v91, v14\n"
    "v_perm_b32 v27, v91, v91, v15\n"
    "v_perm_b32 v28, v91, v91, v16\n"
    "v_perm_b32 v29, v91, v91, v17\n"
    "s_waitcnt vmcnt(8)\n"
    "s_cbranch_vccz .Lfd_qa70\n"
    "s_waitcnt vmcnt(5)\n"
    ".Lfd_qa70:\n"
    "global_load_dwordx4 a[132:135], v9, s[12:13] offset:576 nt\n"
    "v_mfma_f32_16x16x32_bf16 v[30:33], v[26:29], a[92:95], v[30:33]\n"
    "v_perm_b32 v22, v92, v92, v10\n"
    "v_perm_b32 v23, v92, v92, v11\n"
    "v_perm_b32 v24, v92, v92, v12\n"
    "v_perm_b32 v25, v92, v92, v13\n"
    "s_waitcnt vmcnt(8)\n"
    "s_cbranch_vccz .Lfd_qa71\n"
    "s_waitcnt vmcnt(5)\n"
    ".Lfd_qa71:\n"
    "global_load_dwordx4 a[136:139], v9, s[12:13] offset:1728 nt\n"
    "v_mfma_f32_16x16x32_bf16 v[30:33], v[22:25], a[96:99], v[30:33]\n"
    "v_perm_b32 v26, v92, v92, v14\n"
    "v_perm_b32 v27, v92, v92, v15\n"
    "v_perm_b32 v28, v92, v92, v16\n"
    "v_perm_b32 v29, v92, v92, v17\n"
    "s_waitcnt vmcnt(8)\n"
    "s_nop 0\n"
    "v_mfma_f32_16x16x32_bf16 v[30:33], v[26:29], a[100:103], v[30:33]\n"
    "v_perm_b32 v22, v93, v93, v10\n"
    "v_perm_b32 v23, v93, v93, v11\n"
    "v_perm_b32 v24, v93, v93, v12\n"
    "v_perm_b32 v25, v93, v93, v13\n"
    "s_waitcnt vmcnt(7)\n"
    "s_nop 0\n"
    "v_mfma_f32_16x16x32_bf16 v[30:33], v[22:25], a[104:107], v[30:33]\n"
    "v_perm_b32 v26, v93, v93, v14\n"
    "v_perm_b32 v27, v93, v93, v15\n"
    "v_perm_b32 v28, v93, v93, v16\n"
    "v_perm_b32 v29, v93, v93, v17\n"
    "s_waitcnt vmcnt(6)\n"
    "s_nop 0\n"
    "v_mfma_f32_16x16x32_bf16 v[30:33], v[26:29], a[108:111], v[30:33]\n"
    "global_load_dwordx4 a[140:143], v9, s[12:13] offset:2880 nt\n"
    "global_load_dwordx4 a[144:147], v9, s[12:13] offset:4032 nt\n"
    "s_nop 5\n"
    "ds_write_b128 v44, v[30:33] offset:5120\n"
    "s_waitcnt lgkmcnt(0)\n"
    "s_barrier\n"
    "s_waitcnt vmcnt(0)\n"
    "s_barrier\n"
    "ds_read_b128 v[18:21], v45 offset:1664\n"
    "s_waitcnt lgkmcnt(0)\n"
    "v_perm_b32 v22, v18, v18, v10\n"
    "v_perm_b32 v23, v18, v18, v11\n"
    "v_perm_b32 v24, v18, v18, v12\n"
    "v_perm_b32 v25, v18, v18, v13\n"
    "v_perm_b32 v26, v18, v18, v14\n"
    "v_perm_b32 v27, v18, v18, v15\n"
    "v_perm_b32 v28, v18, v18, v16\n"
    "v_perm_b32 v29, v18, v18, v17\n"
    "v_mfma_f32_16x16x32_bf16 v[30:33], v[22:25], a[116:119], 0\n"
    "v_perm_b32 v22, v19, v19, v10\n"
    "v_perm_b32 v23, v19, v19, v11\n"
    "v_perm_b32 v24, v19, v19, v12\n"
    "v_perm_b32 v25, v19, v19, v13\n"
    "v_mfma_f32_16x16x32_bf16 v[30:33], v[26:29], a[120:123], v[30:33]\n"
    "v_perm_b32 v26, v19, v19, v14\n"
    "v_perm_b32 v27, v19, v19, v15\n"
    "v_perm_b32 v28, v19, v19, v16\n"
    "v_perm_b32 v29, v19, v19, v17\n"
    "v_mfma_f32_16x16x32_bf16 v[30:33], v[22:25], a[124:127], v[30:33]\n"
    "v_perm_b32 v22, v20, v20, v10\n"
    "v_perm_b32 v23, v20, v20, v11\n"
    "v_perm_b32 v24, v20, v20, v12\n"
    "v_perm_b32 v25, v20, v20, v13\n"
    "v_mfma_f32_16x16x32_bf16 v[30:33], v[26:29], a[128:131], v[30:33]\n"
    "v_perm_b32 v26, v20, v20, v14\n"
    "v_perm_b32 v27, v20, v20, v15\n"
    "v_perm_b32 v28, v20, v20, v16\n"
    "v_perm_b32 v29, v20, v20, v17\n"
    "v_mfma_f32_16x16x32_bf16 v[30:33], v[22:25], a[132:135], v[30:33]\n"
    "v_perm_b32 v22, v21, v21, v10\n"
    "v_perm_b32 v23, v21, v21, v11\n"
    "v_perm_b32 v24, v21, v21, v12\n"
    "v_perm_b32 v25, v21, v21, v13\n"
    "v_mfma_f32_16x16x32_bf16 v[30:33], v[26:29], a[136:139], v[30:33]\n"
    "v_perm_b32 v26, v21, v21, v14\n"
    "v_perm_b32 v27, v21, v21, v15\n"
    "v_perm_b32 v28, v21, v21, v16\n"
    "v_perm_b32 v29, v21, v21, v17\n"
    "v_mfma_f32_16x16x32_bf16 v[30:33], v[22:25], a[140:143], v[30:33]\n"
    "s_nop 0\n"
    "v_mfma_f32_16x16x32_bf16 v[30:33], v[26:29], a[144:147], v[30:33]\n"
    "s_nop 7\n"
    "v_permlane32_swap_b32 v30, v32\n"
    "v_permlane32_swap_b32 v31, v33\n"
    "s_nop 0\n"
    "v_add_f32_dpp v30, v32, v30 row_ror:8 row_mask:0xf bank_mask:0xf\n"
    "v_add_f32_dpp v31, v33, v31 row_ror:8 row_mask:0xf bank_mask:0xf\n"
    "s_mov_b64 exec, s[46:47]\n"
    "v_add_u32 v30, 0x200, v30\n"
    "v_add_u32 v31, 0x200, v31\n"
    "v_bfi_b32 v30, v97, v95, v30\n"
    "v_bfi_b32 v31, v97, v96, v31\n"
    "global_store_dwordx2 v98, v[30:31], s[22:23] sc1\n"
    "s_endpgm\n"
    ".Lfd_ac2:\n"
    "s_sleep 38\n"
    "global_load_dwordx4 v[16:19], v9, s[22:23] offset:-2048 sc1\n"
    "global_load_dwordx4 v[20:23], v9, s[22:23] offset:-1024 sc1\n"
    "global_load_dwordx4 v[24:27], v9, s[22:23] offset:0 sc1\n"
    "global_load_dwordx4 v[28:31], v9, s[22:23] offset:1024 sc1\n"
    "global_load_dwordx4 v[32:35], v9, s[22:23] offset:2048 sc1\n"
    "global_load_dwordx4 v[36:39], v9, s[22:23] offset:3072 sc1\n"
    "s_mov_b64 s[46:47], -1\n"
    "s_mov_b64 s[48:49], -1\n"
    "s_mov_b64 s[50:51], -1\n"
    "s_mov_b64 s[52:53], -1\n"
    "s_mov_b64 s[54:55], -1\n"
    "s_mov_b64 s[56:57], -1\n"
    "s_mov_b32 s44, 131072\n"
    ".Lfd_qpoll:\n"
    "s_waitcnt vmcnt(0)\n"
    "s_mov_b64 exec, s[46:47]\n"
    "s_cbranch_execz .Lfd_qn0\n"
    "v_xor_b32 v16, s16, v16\n"
    "v_xor_b32 v17, s17, v17\n"
    "v_xor_b32 v18, s18, v18\n"
    "v_xor_b32 v19, s19, v19\n"
    "v_or3_b32 v47, v16, v17, v18\n"
    "v_bitop3_b32 v47, v47, v19, 63 bitop3:0xa8\n"
    "v_cmp_ne_u32 vcc, 0, v47\n"
    "s_wqm_b64 s[46:47], vcc\n"
    "s_mov_b64 exec, s[46:47]\n"
    "s_cbranch_execz .Lfd_qn0\n"
    "global_load_dwordx4 v[16:19], v9, s[22:23] offset:-2048 sc1\n"
    ".Lfd_qn0:\n"
    "s_mov_b64 exec, s[48:49]\n"
    "s_cbranch_execz .Lfd_qn1\n"
    "v_xor_b32 v20, s16, v20\n"
    "v_xor_b32 v21, s17, v21\n"
    "v_xor_b32 v22, s18, v22\n"
    "v_xor_b32 v23, s19, v23\n"
    "v_or3_b32 v47, v20, v21, v22\n"
    "v_bitop3_b32 v47, v47, v23, 63 bitop3:0xa8\n"
    "v_cmp_ne_u32 vcc, 0, v47\n"
    "s_wqm_b64 s[48:49], vcc\n"
    "s_mov_b64 exec, s[48:49]\n"
    "s_cbranch_execz .Lfd_qn1\n"
    "global_load_dwordx4 v[20:23], v9, s[22:23] offset:-1024 sc1\n"
    ".Lfd_qn1:\n"
    "s_mov_b64 exec, s[50:51]\n"
    "s_cbranch_execz .Lfd_qn2\n"
    "v_xor_b32 v24, s16, v24\n"
    "v_xor_b32 v25, s17, v25\n"
    "v_xor_b32 v26, s18, v26\n"
    "v_xor_b32 v27, s19, v27\n"
    "v_or3_b32 v47, v24, v25, v26\n"
    "v_bitop3_b32 v47, v47, v27, 63 bitop3:0xa8\n"
    "v_cmp_ne_u32 vcc, 0, v47\n"
    "s_wqm_b64 s[50:51], vcc\n"
    "s_mov_b64 exec, s[50:51]\n"
    "s_cbranch_execz .Lfd_qn2\n"
    "global_load_dwordx4 v[24:27], v9, s[22:23] offset:0 sc1\n"
    ".Lfd_qn2:\n"
    "s_mov_b64 exec, s[52:53]\n"
    "s_cbranch_execz .Lfd_qn3\n"
    "v_xor_b32 v28, s16, v28\n"
    "v_xor_b32 v29, s17, v29\n"
    "v_xor_b32 v30, s18, v30\n"
    "v_xor_b32 v31, s19, v31\n"
    "v_or3_b32 v47, v28, v29, v30\n"
    "v_bitop3_b32 v47, v47, v31, 63 bitop3:0xa8\n"
    "v_cmp_ne_u32 vcc, 0, v47\n"
    "s_wqm_b64 s[52:53], vcc\n"
    "s_mov_b64 exec, s[52:53]\n"
    "s_cbranch_execz .Lfd_qn3\n"
    "global_load_dwordx4 v[28:31], v9, s[22:23] offset:1024 sc1\n"
    ".Lfd_qn3:\n"
    "s_mov_b64 exec, s[54:55]\n"
    "s_cbranch_execz .Lfd_qn4\n"
    "v_xor_b32 v32, s16, v32\n"
    "v_xor_b32 v33, s17, v33\n"
    "v_xor_b32 v34, s18, v34\n"
    "v_xor_b32 v35, s19, v35\n"
    "v_or3_b32 v47, v32, v33, v34\n"
    "v_bitop3_b32 v47, v47, v35, 63 bitop3:0xa8\n"
    "v_cmp_ne_u32 vcc, 0, v47\n"
    "s_wqm_b64 s[54:55], vcc\n"
    "s_mov_b64 exec, s[54:55]\n"
    "s_cbranch_execz .Lfd_qn4\n"
    "global_load_dwordx4 v[32:35], v9, s[22:23] offset:2048 sc1\n"
    ".Lfd_qn4:\n"
    "s_mov_b64 exec, s[56:57]\n"
    "s_cbranch_execz .Lfd_qn5\n"
    "v_xor_b32 v36, s16, v36\n"
    "v_xor_b32 v37, s17, v37\n"
    "v_xor_b32 v38, s18, v38\n"
    "v_xor_b32 v39, s19, v39\n"
    "v_or3_b32 v47, v36, v37, v38\n"
    "v_bitop3_b32 v47, v47, v39, 63 bitop3:0xa8\n"
    "v_cmp_ne_u32 vcc, 0, v47\n"
    "s_wqm_b64 s[56:57], vcc\n"
    "s_mov_b64 exec, s[56:57]\n"
    "s_cbranch_execz .Lfd_qn5\n"
    "global_load_dwordx4 v[36:39], v9, s[22:23] offset:3072 sc1\n"
    ".Lfd_qn5:\n"
    "s_or_b64 vcc, s[46:47], s[48:49]\n"
    "s_or_b64 vcc, vcc, s[50:51]\n"
    "s_or_b64 vcc, vcc, s[52:53]\n"
    "s_or_b64 vcc, vcc, s[54:55]\n"
    "s_or_b64 vcc, vcc, s[56:57]\n"
    "s_cbranch_scc0 .Lfd_qok\n"
    "s_sub_u32 s44, s44, 1\n"
    "s_cbranch_scc0 .Lfd_qpoll\n"
    "s_waitcnt vmcnt(0)\n"
    ".Lfd_qok:\n"
    "s_mov_b64 exec, -1\n"
    "v_lshlrev_b32 v10, 3, v0\n"
    "v_pk_add_f32 v[16:17], v[16:17], v[20:21]\n"
    "v_pk_add_f32 v[18:19], v[18:19], v[22:23]\n"
    "v_pk_add_f32 v[24:25], v[24:25], v[28:29]\n"
    "v_pk_add_f32 v[26:27], v[26:27], v[30:31]\n"
    "v_pk_add_f32 v[32:33], v[32:33], v[36:37]\n"
    "v_pk_add_f32 v[34:35], v[34:35], v[38:39]\n"
    "v_pk_add_f32 v[16:17], v[16:17], v[24:25]\n"
    "v_pk_add_f32 v[18:19], v[18:19], v[26:27]\n"
    "v_pk_add_f32 v[16:17], v[16:17], v[32:33]\n"
    "v_pk_add_f32 v[18:19], v[18:19], v[34:35]\n"
    "v_cvt_pk_bf16_f32 v16, v16, v17\n"
    "v_cvt_pk_bf16_f32 v17, v18, v19\n"
    "ds_write_b64 v10, v[16:17] offset:1152\n"
    "s_waitcnt lgkmcnt(0)\n"
    "s_barrier\n"
    "s_mul_i32 s34, s63, 294912\n"
    "s_add_u32 s12, s12, s34\n"
    "s_addc_u32 s13, s13, 0\n"
    "s_load_dword s14, s[12:13], 0\n"
    "s_load_dword s15, s[12:13], 294908\n"
    "s_mul_i32 s36, s31, 13824\n"
    "s_add_u32 s36, s36, 397312\n"
    "s_mul_i32 s37, s30, 1152\n"
    "s_add_u32 s37, s37, s36\n"
    "v_add_u32 v8, s37, v3\n"
    "s_mul_i32 s36, s63, 13824\n"
    "s_add_u32 s36, s36, 397312\n"
    "v_lshrrev_b32 v12, 4, v0\n"
    "v_mul_u32_u24 v10, 1152, v12\n"
    "v_add_u32 v10, s36, v10\n"
    "s_and_b32 s38, s30, 3\n"
    "s_lshl_b32 s39, s38, 8\n"
    "v_and_b32 v13, 15, v0\n"
    "v_lshl_add_u32 v9, v13, 4, s39\n"
    "v_add_u32 v40, v9, v10\n"
    "v_add_u32 v41, 4608, v40\n"
    "v_add_u32 v42, 9216, v40\n"
    "v_add_u32 v43, 0x400, v10\n"
    "v_add_u32 v44, 4608, v43\n"
    "v_add_u32 v45, 9216, v43\n"
    "v_lshlrev_b32 v11, 1, v12\n"
    "v_lshl_add_u32 v11, v13, 3, v11\n"
    "v_lshlrev_b32 v12, 5, v14\n"
    "v_mov_b32 v46, 0x3ff\n"
    "s_mov_b32 s46, 0x3fb8aa3b\n"
    "s_barrier\n"
    "ds_read_b128 v[16:19], v12 offset:1856\n"
    "ds_read_b64 v[20:21], v12 offset:1872\n"
    "s_waitcnt lgkmcnt(0)\n"
    "v_max3_f32 v22, v16, v18, v20\n"
    "v_sub_f32 v16, v16, v22\n"
    "v_mul_f32 v16, s46, v16\n"
    "v_exp_f32 v48, v16\n"
    "v_sub_f32 v18, v18, v22\n"
    "v_mul_f32 v18, s46, v18\n"
    "v_exp_f32 v50, v18\n"
    "v_sub_f32 v20, v20, v22\n"
    "v_mul_f32 v20, s46, v20\n"
    "v_exp_f32 v52, v20\n"
    "v_mul_f32 v23, v48, v17\n"
    "v_fmac_f32 v23, v50, v19\n"
    "v_fmac_f32 v23, v52, v21\n"
    "v_mov_b32 v24, v22\n"
    "v_mov_b32 v25, v23\n"
    "s_nop 0\n"
    "v_permlane32_swap_b32 v22, v24\n"
    "v_permlane32_swap_b32 v23, v25\n"
    "v_add_u32 v22, 0x200, v22\n"
    "v_add_u32 v23, 0x200, v23\n"
    "v_add_u32 v24, 0x200, v24\n"
    "v_add_u32 v25, 0x200, v25\n"
    "v_bfi_b32 v22, v46, s40, v22\n"
    "v_bfi_b32 v23, v46, s41, v23\n"
    "v_bfi_b32 v24, v46, s42, v24\n"
    "v_bfi_b32 v25, v46, s43, v25\n"
    "s_mov_b64 exec, 1\n"
    "global_store_dwordx4 v8, v[22:25], s[22:23] offset:1024 sc1\n"
    "s_mov_b64 exec, -1\n"
    "s_barrier\n"
    "ds_read_b128 v[16:19], v3 offset:5120\n"
    "ds_read_b128 v[20:23], v3 offset:6144\n"
    "ds_read_b128 v[24:27], v3 offset:7168\n"
    "s_waitcnt lgkmcnt(2)\n"
    "v_pk_mul_f32 v[16:17], v[16:17], v[48:49] op_sel_hi:[1,0]\n"
    "v_pk_mul_f32 v[18:19], v[18:19], v[48:49] op_sel_hi:[1,0]\n"
    "s_waitcnt lgkmcnt(1)\n"
    "v_pk_fma_f32 v[16:17], v[20:21], v[50:51], v[16:17] op_sel_hi:[1,0,1]\n"
    "v_pk_fma_f32 v[18:19], v[22:23], v[50:51], v[18:19] op_sel_hi:[1,0,1]\n"
    "s_waitcnt lgkmcnt(0)\n"
    "v_pk_fma_f32 v[16:17], v[24:25], v[52:53], v[16:17] op_sel_hi:[1,0,1]\n"
    "v_pk_fma_f32 v[18:19], v[26:27], v[52:53], v[18:19] op_sel_hi:[1,0,1]\n"
    "v_add_u32 v16, 0x200, v16\n"
    "v_add_u32 v17, 0x200, v17\n"
    "v_add_u32 v18, 0x200, v18\n"
    "v_add_u32 v19, 0x200, v19\n"
    "v_bfi_b32 v16, v46, s40, v16\n"
    "v_bfi_b32 v17, v46, s41, v17\n"
    "v_bfi_b32 v18, v46, s42, v18\n"
    "v_bfi_b32 v19, v46, s43, v19\n"
    "global_store_dwordx4 v8, v[16:19], s[22:23] sc1\n"
    ".Lfd_owait:\n"
    "s_memrealtime s[98:99]\n"
    "s_waitcnt lgkmcnt(0)\n"
    "s_sub_u32 s98, s98, s96\n"
    "s_cmpk_lt_u32 s98, 775\n"
    "s_cbranch_scc1 .Lfd_owait\n"
    "s_mov_b32 s44, 131072\n"
    ".Lfd_spoll:\n"
    "global_load_dwordx4 v[56:59], v43, s[22:23] sc1\n"
    "global_load_dwordx4 v[60:63], v44, s[22:23] sc1\n"
    "global_load_dwordx4 v[64:67], v45, s[22:23] sc1\n"
    "global_load_dwordx4 v[20:23], v40, s[22:23] sc1\n"
    "global_load_dwordx4 v[24:27], v41, s[22:23] sc1\n"
    "global_load_dwordx4 v[28:31], v42, s[22:23] sc1\n"
    ".Lfd_spoll1:\n"
    "s_waitcnt vmcnt(3)\n"
    "v_xor_b32 v56, s40, v56\n"
    "v_xor_b32 v57, s41, v57\n"
    "v_xor_b32 v58, s42, v58\n"
    "v_xor_b32 v59, s43, v59\n"
    "v_or3_b32 v47, v56, v57, v58\n"
    "v_bitop3_b32 v47, v47, v59, s45 bitop3:0xa8\n"
    "v_cmp_ne_u32 s[48:49], 0, v47\n"
    "v_xor_b32 v60, s40, v60\n"
    "v_xor_b32 v61, s41, v61\n"
    "v_xor_b32 v62, s42, v62\n"
    "v_xor_b32 v63, s43, v63\n"
    "v_or3_b32 v47, v60, v61, v62\n"
    "v_bitop3_b32 v47, v47, v63, s45 bitop3:0xa8\n"
    "v_cmp_ne_u32 s[50:51], 0, v47\n"
    "v_xor_b32 v64, s40, v64\n"
    "v_xor_b32 v65, s41, v65\n"
    "v_xor_b32 v66, s42, v66\n"
    "v_xor_b32 v67, s43, v67\n"
    "v_or3_b32 v47, v64, v65, v66\n"
    "v_bitop3_b32 v47, v47, v67, s45 bitop3:0xa8\n"
    "v_cmp_ne_u32 s[52:53], 0, v47\n"
    "s_or_b64 vcc, s[48:49], s[50:51]\n"
    "s_or_b64 vcc, vcc, s[52:53]\n"
    "s_cbranch_scc0 .Lfd_sok\n"
    "s_waitcnt vmcnt(0)\n"
    "s_sub_u32 s44, s44, 1\n"
    "s_cbranch_scc0 .Lfd_spoll\n"
    ".Lfd_sok:\n"
    "s_bitcmp1_b32 s38, 1\n"
    "s_cbranch_scc0 .Lfd_sh0\n"
    "v_mov_b64 v[56:57], v[58:59]\n"
    "v_mov_b64 v[60:61], v[62:63]\n"
    "v_mov_b64 v[64:65], v[66:67]\n"
    ".Lfd_sh0:\n"
    "v_max3_f32 v32, v56, v60, v64\n"
    "v_mov_b32 v33, v32\n"
    "s_nop 1\n"
    "v_permlane16_swap_b32 v32, v33\n"
    "v_max_f32 v32, v32, v33\n"
    "v_mov_b32 v33, v32\n"
    "s_nop 1\n"
    "v_permlane32_swap_b32 v32, v33\n"
    "v_max_f32 v32, v32, v33\n"
    "v_sub_f32 v33, v56, v32\n"
    "v_mul_f32 v33, s46, v33\n"
    "v_exp_f32 v48, v33\n"
    "v_sub_f32 v33, v60, v32\n"
    "v_mul_f32 v33, s46, v33\n"
    "v_exp_f32 v50, v33\n"
    "v_sub_f32 v33, v64, v32\n"
    "v_mul_f32 v33, s46, v33\n"
    "v_exp_f32 v52, v33\n"
    "v_mul_f32 v32, v48, v57\n"
    "v_fmac_f32 v32, v50, v61\n"
    "v_fmac_f32 v32, v52, v65\n"
    "v_mov_b32 v33, v32\n"
    "s_nop 1\n"
    "v_permlane16_swap_b32 v32, v33\n"
    "v_add_f32 v32, v32, v33\n"
    "v_mov_b32 v33, v32\n"
    "s_nop 1\n"
    "v_permlane32_swap_b32 v32, v33\n"
    "v_add_f32 v32, v32, v33\n"
    "v_rcp_f32 v32, v32\n"
    "s_mov_b64 s[48:49], -1\n"
    "s_mov_b64 s[50:51], -1\n"
    "s_mov_b64 s[52:53], -1\n"
    "v_mul_f32 v48, v48, v32\n"
    "v_mul_f32 v50, v50, v32\n"
    "v_mul_f32 v52, v52, v32\n"
    "s_mov_b32 s44, 131072\n"
    ".Lfd_opoll:\n"
    "s_waitcnt vmcnt(0)\n"
    "s_mov_b64 exec, s[48:49]\n"
    "s_cbranch_execz .Lfd_on0\n"
    "v_xor_b32 v20, s40, v20\n"
    "v_xor_b32 v21, s41, v21\n"
    "v_xor_b32 v22, s42, v22\n"
    "v_xor_b32 v23, s43, v23\n"
    "v_or3_b32 v47, v20, v21, v22\n"
    "v_bitop3_b32 v47, v47, v23, s45 bitop3:0xa8\n"
    "v_cmp_ne_u32 s[48:49], 0, v47\n"
    "s_mov_b64 exec, s[48:49]\n"
    "s_cbranch_execz .Lfd_on0\n"
    "global_load_dwordx4 v[20:23], v40, s[22:23] sc1\n"
    ".Lfd_on0:\n"
    "s_mov_b64 exec, s[50:51]\n"
    "s_cbranch_execz .Lfd_on1\n"
    "v_xor_b32 v24, s40, v24\n"
    "v_xor_b32 v25, s41, v25\n"
    "v_xor_b32 v26, s42, v26\n"
    "v_xor_b32 v27, s43, v27\n"
    "v_or3_b32 v47, v24, v25, v26\n"
    "v_bitop3_b32 v47, v47, v27, s45 bitop3:0xa8\n"
    "v_cmp_ne_u32 s[50:51], 0, v47\n"
    "s_mov_b64 exec, s[50:51]\n"
    "s_cbranch_execz .Lfd_on1\n"
    "global_load_dwordx4 v[24:27], v41, s[22:23] sc1\n"
    ".Lfd_on1:\n"
    "s_mov_b64 exec, s[52:53]\n"
    "s_cbranch_execz .Lfd_on2\n"
    "v_xor_b32 v28, s40, v28\n"
    "v_xor_b32 v29, s41, v29\n"
    "v_xor_b32 v30, s42, v30\n"
    "v_xor_b32 v31, s43, v31\n"
    "v_or3_b32 v47, v28, v29, v30\n"
    "v_bitop3_b32 v47, v47, v31, s45 bitop3:0xa8\n"
    "v_cmp_ne_u32 s[52:53], 0, v47\n"
    "s_mov_b64 exec, s[52:53]\n"
    "s_cbranch_execz .Lfd_on2\n"
    "global_load_dwordx4 v[28:31], v42, s[22:23] sc1\n"
    ".Lfd_on2:\n"
    "s_or_b64 vcc, s[48:49], s[50:51]\n"
    "s_or_b64 vcc, vcc, s[52:53]\n"
    "s_cbranch_scc0 .Lfd_ook\n"
    "s_sub_u32 s44, s44, 1\n"
    "s_cbranch_scc0 .Lfd_opoll\n"
    "s_waitcnt vmcnt(0)\n"
    ".Lfd_ook:\n"
    "s_mov_b64 exec, -1\n"
    "v_pk_mul_f32 v[16:17], v[20:21], v[48:49] op_sel_hi:[1,0]\n"
    "v_pk_mul_f32 v[18:19], v[22:23], v[48:49] op_sel_hi:[1,0]\n"
    "v_pk_fma_f32 v[16:17], v[24:25], v[50:51], v[16:17] op_sel_hi:[1,0,1]\n"
    "v_pk_fma_f32 v[18:19], v[26:27], v[50:51], v[18:19] op_sel_hi:[1,0,1]\n"
    "v_pk_fma_f32 v[16:17], v[28:29], v[52:53], v[16:17] op_sel_hi:[1,0,1]\n"
    "v_pk_fma_f32 v[18:19], v[30:31], v[52:53], v[18:19] op_sel_hi:[1,0,1]\n"
    "s_nop 1\n"
    "v_permlane32_swap_b32 v16, v18\n"
    "v_permlane32_swap_b32 v17, v19\n"
    "v_pk_add_f32 v[16:17], v[16:17], v[18:19]\n"
    "s_nop 1\n"
    "v_permlane16_swap_b32 v16, v17\n"
    "v_add_f32 v16, v16, v17\n"
    "v_cvt_pk_bf16_f32 v16, v16, v16\n"
    "ds_write_b16 v11, v16 offset:1664\n"
    "s_waitcnt lgkmcnt(0)\n"
    "s_barrier\n"
    "s_endpgm\n"
    ".Lfd_fc:\n"
    "s_and_b32 s34, s29, 1\n"
    "s_lshl1_add_u32 s34, s28, s34\n"
    "s_lshl4_add_u32 s34, s34, 15\n"
    "s_lshl_b32 s34, s34, 20\n"
    "s_add_u32 s10, s8, s34\n"
    "s_addc_u32 s11, s9, 0\n"
    "s_load_dword s12, s[10:11], 0\n"
    "s_load_dword s13, s[10:11], 0xffffc\n"
    "s_sub_u32 s36, s29, 24\n"
    "s_mul_i32 s37, s28, 36864\n"
    "s_add_u32 s37, s37, 104704\n"
    "s_lshl3_add_u32 s38, s36, s36\n"
    "v_and_b32 v8, 31, v0\n"
    "v_cmp_lt_u32 vcc, 8, v8\n"
    "v_add_u32 v9, -9, v8\n"
    "s_nop 0\n"
    "v_cndmask_b32 v8, v8, v9, vcc\n"
    "v_cndmask_b32 v10, 0, 1, vcc\n"
    "v_lshlrev_b32 v11, 4, v8\n"
    "v_lshl_add_u32 v11, v10, 3, v11\n"
    "v_lshl_add_u32 v11, v14, 2, v11\n"
    "v_add_u32 v8, s38, v8\n"
    "v_lshrrev_b32 v9, 3, v8\n"
    "v_lshl_add_u32 v9, v9, 1, v14\n"
    "v_lshl_add_u32 v9, v9, 1, v10\n"
    "v_and_b32 v8, 7, v8\n"
    "v_lshl_add_u32 v9, v9, 3, v8\n"
    "v_lshl_add_u32 v32, v9, 4, s37\n"
    "v_add_u32 v33, 9216, v32\n"
    "v_add_u32 v34, 9216, v33\n"
    "v_add_u32 v35, 9216, v34\n"
    "v_and_b32 v8, 1, v0\n"
    "v_cmp_eq_u32 vcc, 1, v8\n"
    "v_mov_b32 v36, s24\n"
    "v_mov_b32 v8, s26\n"
    "v_cndmask_b32 v36, v36, v8, vcc\n"
    "v_mov_b32 v37, s25\n"
    "v_mov_b32 v8, s27\n"
    "v_cndmask_b32 v37, v37, v8, vcc\n"
    "v_mov_b32 v38, 0x3ff\n"
    "s_lshl3_add_u32 s39, s36, s28\n"
    "s_min_u32 s58, s39, 35\n"
    "s_lshr_b32 s59, s58, 1\n"
    "s_lshl2_add_u32 s59, s59, 0\n"
    "s_lshl_b32 s59, s59, 12\n"
    "s_and_b32 s60, s58, 1\n"
    "s_lshl_b32 s60, s60, 6\n"
    "s_add_u32 s59, s59, s60\n"
    "s_add_u32 s59, s59, 618496\n"
    "v_lshrrev_b32 v8, 3, v0\n"
    "v_lshl_add_u32 v170, v8, 7, s59\n"
    "v_and_b32 v8, 7, v0\n"
    "v_lshl_add_u32 v170, v8, 3, v170\n"
    "v_lshrrev_b32 v171, 5, v0\n"
    "v_lshl_add_u32 v171, v171, 3, v8\n"
    "s_lshl_b32 s59, 0, 2\n"
    "v_lshl_add_u32 v171, v171, 4, s59\n"
    "s_movk_i32 s45, 0x3ff\n"
    "v_and_b32 v172, s45, v36\n"
    "v_and_b32 v173, s45, v37\n"
    "v_mov_b64 v[174:175], 0\n"
    "s_mov_b32 s61, 0\n"
    "s_lshr_b32 s46, s58, 2\n"
    "s_lshl_b32 s46, s46, 6\n"
    "s_and_b32 s35, s58, 1\n"
    "s_lshl2_add_u32 s46, s35, s46\n"
    "s_and_b32 s35, s58, 2\n"
    "s_add_u32 s46, s46, s35\n"
    "v_and_b32 v8, 7, v0\n"
    "v_lshl_add_u32 v8, v8, 3, s46\n"
    "v_bfe_u32 v9, v0, 3, 1\n"
    "v_add_lshl_u32 v42, v8, v9, 1\n"
    "global_load_ushort v43, v42, s[4:5]\n"
    "s_mov_b32 s46, 0x3ffff\n"
    "s_mov_b32 s47, 0x3ffff\n"
    "s_mov_b64 s[48:49], s[46:47]\n"
    "s_mov_b64 s[50:51], s[46:47]\n"
    "s_mov_b64 s[52:53], s[46:47]\n"
    "s_mov_b64 s[54:55], s[46:47]\n"
    "s_mov_b64 s[56:57], s[46:47]\n"
    "s_mov_b64 s[58:59], s[46:47]\n"
    "s_mov_b64 s[60:61], s[46:47]\n"
    "s_sleep 0\n"
    "s_load_dword s9, s[16:17], 0\n"
    "s_mov_b32 s6, 0x200000\n"
    "s_load_dword s14, s[16:17], s6\n"
    "s_mov_b32 s7, 0x400000\n"
    "s_load_dword s15, s[16:17], s7\n"
    "s_mov_b32 s8, 0x500000\n"
    "s_load_dword s98, s[16:17], s8\n"
    "s_sleep 20\n"
    "s_lshl3_add_u32 s34, s28, s36\n"
    "s_mul_i32 s34, s34, 82944\n"
    "s_add_u32 s18, s18, s34\n"
    "s_addc_u32 s19, s19, 0\n"
    "s_waitcnt lgkmcnt(0)\n"
    "s_load_dword s14, s[18:19], 0\n"
    "s_load_dword s15, s[18:19], 82940\n"
    "s_barrier\n"
    "s_sleep 10\n"
    ".Lfd_gwait:\n"
    "s_memrealtime s[98:99]\n"
    "s_waitcnt lgkmcnt(0)\n"
    "s_sub_u32 s98, s98, s96\n"
    "s_cmpk_lt_u32 s98, 730\n"
    "s_cbranch_scc1 .Lfd_gwait\n"
    "s_mov_b64 exec, s[46:47]\n"
    "global_load_dwordx4 v[48:51], v32, s[22:23] offset:-2304 sc1\n"
    "global_load_dwordx4 v[52:55], v32, s[22:23] offset:2304 sc1\n"
    "global_load_dwordx4 v[56:59], v33, s[22:23] offset:-2304 sc1\n"
    "global_load_dwordx4 v[60:63], v33, s[22:23] offset:2304 sc1\n"
    "global_load_dwordx4 v[64:67], v34, s[22:23] offset:-2304 sc1\n"
    "global_load_dwordx4 v[68:71], v34, s[22:23] offset:2304 sc1\n"
    "global_load_dwordx4 v[72:75], v35, s[22:23] offset:-2304 sc1\n"
    "global_load_dwordx4 v[76:79], v35, s[22:23] offset:2304 sc1\n"
    "s_mov_b32 s44, 131072\n"
    ".Lfd_gpoll:\n"
    "s_waitcnt vmcnt(0)\n"
    "s_mov_b64 exec, s[46:47]\n"
    "s_cbranch_execz .Lfd_gn0\n"
    "v_xor_b32 v48, s40, v48\n"
    "v_xor_b32 v49, s41, v49\n"
    "v_xor_b32 v50, s42, v50\n"
    "v_xor_b32 v51, s43, v51\n"
    "v_or3_b32 v47, v48, v49, v50\n"
    "v_bitop3_b32 v47, v47, v51, s45 bitop3:0xa8\n"
    "v_cmp_ne_u32 s[46:47], 0, v47\n"
    "s_mov_b64 exec, s[46:47]\n"
    "s_cbranch_execz .Lfd_gn0\n"
    "global_load_dwordx4 v[48:51], v32, s[22:23] offset:-2304 sc1\n"
    ".Lfd_gn0:\n"
    "s_mov_b64 exec, s[48:49]\n"
    "s_cbranch_execz .Lfd_gn1\n"
    "v_xor_b32 v52, s40, v52\n"
    "v_xor_b32 v53, s41, v53\n"
    "v_xor_b32 v54, s42, v54\n"
    "v_xor_b32 v55, s43, v55\n"
    "v_or3_b32 v47, v52, v53, v54\n"
    "v_bitop3_b32 v47, v47, v55, s45 bitop3:0xa8\n"
    "v_cmp_ne_u32 s[48:49], 0, v47\n"
    "s_mov_b64 exec, s[48:49]\n"
    "s_cbranch_execz .Lfd_gn1\n"
    "global_load_dwordx4 v[52:55], v32, s[22:23] offset:2304 sc1\n"
    ".Lfd_gn1:\n"
    "s_mov_b64 exec, s[50:51]\n"
    "s_cbranch_execz .Lfd_gn2\n"
    "v_xor_b32 v56, s40, v56\n"
    "v_xor_b32 v57, s41, v57\n"
    "v_xor_b32 v58, s42, v58\n"
    "v_xor_b32 v59, s43, v59\n"
    "v_or3_b32 v47, v56, v57, v58\n"
    "v_bitop3_b32 v47, v47, v59, s45 bitop3:0xa8\n"
    "v_cmp_ne_u32 s[50:51], 0, v47\n"
    "s_mov_b64 exec, s[50:51]\n"
    "s_cbranch_execz .Lfd_gn2\n"
    "global_load_dwordx4 v[56:59], v33, s[22:23] offset:-2304 sc1\n"
    ".Lfd_gn2:\n"
    "s_mov_b64 exec, s[52:53]\n"
    "s_cbranch_execz .Lfd_gn3\n"
    "v_xor_b32 v60, s40, v60\n"
    "v_xor_b32 v61, s41, v61\n"
    "v_xor_b32 v62, s42, v62\n"
    "v_xor_b32 v63, s43, v63\n"
    "v_or3_b32 v47, v60, v61, v62\n"
    "v_bitop3_b32 v47, v47, v63, s45 bitop3:0xa8\n"
    "v_cmp_ne_u32 s[52:53], 0, v47\n"
    "s_mov_b64 exec, s[52:53]\n"
    "s_cbranch_execz .Lfd_gn3\n"
    "global_load_dwordx4 v[60:63], v33, s[22:23] offset:2304 sc1\n"
    ".Lfd_gn3:\n"
    "s_mov_b64 exec, s[54:55]\n"
    "s_cbranch_execz .Lfd_gn4\n"
    "v_xor_b32 v64, s40, v64\n"
    "v_xor_b32 v65, s41, v65\n"
    "v_xor_b32 v66, s42, v66\n"
    "v_xor_b32 v67, s43, v67\n"
    "v_or3_b32 v47, v64, v65, v66\n"
    "v_bitop3_b32 v47, v47, v67, s45 bitop3:0xa8\n"
    "v_cmp_ne_u32 s[54:55], 0, v47\n"
    "s_mov_b64 exec, s[54:55]\n"
    "s_cbranch_execz .Lfd_gn4\n"
    "global_load_dwordx4 v[64:67], v34, s[22:23] offset:-2304 sc1\n"
    ".Lfd_gn4:\n"
    "s_mov_b64 exec, s[56:57]\n"
    "s_cbranch_execz .Lfd_gn5\n"
    "v_xor_b32 v68, s40, v68\n"
    "v_xor_b32 v69, s41, v69\n"
    "v_xor_b32 v70, s42, v70\n"
    "v_xor_b32 v71, s43, v71\n"
    "v_or3_b32 v47, v68, v69, v70\n"
    "v_bitop3_b32 v47, v47, v71, s45 bitop3:0xa8\n"
    "v_cmp_ne_u32 s[56:57], 0, v47\n"
    "s_mov_b64 exec, s[56:57]\n"
    "s_cbranch_execz .Lfd_gn5\n"
    "global_load_dwordx4 v[68:71], v34, s[22:23] offset:2304 sc1\n"
    ".Lfd_gn5:\n"
    "s_mov_b64 exec, s[58:59]\n"
    "s_cbranch_execz .Lfd_gn6\n"
    "v_xor_b32 v72, s40, v72\n"
    "v_xor_b32 v73, s41, v73\n"
    "v_xor_b32 v74, s42, v74\n"
    "v_xor_b32 v75, s43, v75\n"
    "v_or3_b32 v47, v72, v73, v74\n"
    "v_bitop3_b32 v47, v47, v75, s45 bitop3:0xa8\n"
    "v_cmp_ne_u32 s[58:59], 0, v47\n"
    "s_mov_b64 exec, s[58:59]\n"
    "s_cbranch_execz .Lfd_gn6\n"
    "global_load_dwordx4 v[72:75], v35, s[22:23] offset:-2304 sc1\n"
    ".Lfd_gn6:\n"
    "s_mov_b64 exec, s[60:61]\n"
    "s_cbranch_execz .Lfd_gn7\n"
    "v_xor_b32 v76, s40, v76\n"
    "v_xor_b32 v77, s41, v77\n"
    "v_xor_b32 v78, s42, v78\n"
    "v_xor_b32 v79, s43, v79\n"
    "v_or3_b32 v47, v76, v77, v78\n"
    "v_bitop3_b32 v47, v47, v79, s45 bitop3:0xa8\n"
    "v_cmp_ne_u32 s[60:61], 0, v47\n"
    "s_mov_b64 exec, s[60:61]\n"
    "s_cbranch_execz .Lfd_gn7\n"
    "global_load_dwordx4 v[76:79], v35, s[22:23] offset:2304 sc1\n"
    ".Lfd_gn7:\n"
    "s_or_b64 vcc, s[46:47], s[48:49]\n"
    "s_or_b64 vcc, vcc, s[50:51]\n"
    "s_or_b64 vcc, vcc, s[52:53]\n"
    "s_or_b64 vcc, vcc, s[54:55]\n"
    "s_or_b64 vcc, vcc, s[56:57]\n"
    "s_or_b64 vcc, vcc, s[58:59]\n"
    "s_or_b64 vcc, vcc, s[60:61]\n"
    "s_cbranch_scc0 .Lfd_gok\n"
    "s_sub_u32 s44, s44, 1\n"
    "s_cbranch_scc0 .Lfd_gpoll\n"
    "s_waitcnt vmcnt(0)\n"
    ".Lfd_gok:\n"
    "s_mov_b64 exec, -1\n"
    "v_pk_add_f32 v[48:49], v[48:49], v[52:53]\n"
    "v_pk_add_f32 v[50:51], v[50:51], v[54:55]\n"
    "v_pk_add_f32 v[56:57], v[56:57], v[60:61]\n"
    "v_pk_add_f32 v[58:59], v[58:59], v[62:63]\n"
    "v_pk_add_f32 v[64:65], v[64:65], v[68:69]\n"
    "v_pk_add_f32 v[66:67], v[66:67], v[70:71]\n"
    "v_pk_add_f32 v[72:73], v[72:73], v[76:77]\n"
    "v_pk_add_f32 v[74:75], v[74:75], v[78:79]\n"
    "v_pk_add_f32 v[48:49], v[48:49], v[56:57]\n"
    "v_pk_add_f32 v[50:51], v[50:51], v[58:59]\n"
    "v_pk_add_f32 v[64:65], v[64:65], v[72:73]\n"
    "v_pk_add_f32 v[66:67], v[66:67], v[74:75]\n"
    "v_pk_add_f32 v[48:49], v[48:49], v[64:65]\n"
    "v_pk_add_f32 v[50:51], v[50:51], v[66:67]\n"
    "s_nop 1\n"
    "v_permlane32_swap_b32 v48, v50\n"
    "v_permlane32_swap_b32 v49, v51\n"
    "v_cvt_pk_bf16_f32 v17, v48, v49\n"
    "v_cvt_pk_bf16_f32 v19, v50, v51\n"
    "v_lshlrev_b32 v16, 16, v17\n"
    "v_and_b32 v17, 0xffff0000, v17\n"
    "v_lshlrev_b32 v18, 16, v19\n"
    "v_and_b32 v19, 0xffff0000, v19\n"
    "v_mul_f32 v20, 0xbfb8aa3b, v16\n"
    "v_exp_f32 v20, v20\n"
    "s_nop 0\n"
    "v_add_f32 v20, 1.0, v20\n"
    "v_rcp_f32 v20, v20\n"
    "s_nop 0\n"
    "v_mul_f32 v16, v16, v20\n"
    "v_cvt_pk_bf16_f32 v16, v16, v16\n"
    "v_lshlrev_b32 v16, 16, v16\n"
    "v_mul_f32 v21, 0xbfb8aa3b, v17\n"
    "v_exp_f32 v21, v21\n"
    "s_nop 0\n"
    "v_add_f32 v21, 1.0, v21\n"
    "v_rcp_f32 v21, v21\n"
    "s_nop 0\n"
    "v_mul_f32 v17, v17, v21\n"
    "v_cvt_pk_bf16_f32 v17, v17, v17\n"
    "v_lshlrev_b32 v17, 16, v17\n"
    "v_pk_mul_f32 v[16:17], v[16:17], v[18:19]\n"
    "v_cvt_pk_bf16_f32 v16, v16, v17\n"
    "s_mov_b32 exec_lo, 0x3ffff\n"
    "s_mov_b32 exec_hi, 0x3ffff\n"
    "ds_write_b32 v11, v16 offset:1664\n"
    "s_mov_b64 exec, -1\n"
    "s_waitcnt lgkmcnt(0)\n"
    "s_barrier\n"
    "s_barrier\n"
    "s_mov_b32 s61, 0\n"
    "v_lshlrev_b32 v43, 16, v43\n"
    ".Lfd_red:\n"
    "s_cmp_lt_u32 s39, 36\n"
    "s_cbranch_scc0 .Lfd_end\n"
    "s_mov_b64 exec, -1\n"
    "s_mov_b64 s[48:49], -1\n"
    "s_mov_b64 s[50:51], -1\n"
    "s_mov_b64 s[52:53], -1\n"
    "s_mov_b64 s[54:55], -1\n"
    "s_mov_b32 s44, 131072\n"
    "s_or_b64 exec, s[48:49], 1\n"
    "global_atomic_add_x2 v[184:185], v170, v[174:175], s[22:23] offset:0 sc0\n"
    "s_or_b64 exec, s[50:51], 1\n"
    "global_atomic_add_x2 v[186:187], v170, v[174:175], s[22:23] offset:1024 sc0\n"
    "s_or_b64 exec, s[52:53], 1\n"
    "global_atomic_add_x2 v[188:189], v170, v[174:175], s[22:23] offset:2048 sc0\n"
    "s_or_b64 exec, s[54:55], 1\n"
    "global_atomic_add_x2 v[190:191], v170, v[174:175], s[22:23] offset:3072 sc0\n"
    "s_sleep 3\n"
    "s_or_b64 exec, s[48:49], 1\n"
    "global_atomic_add_x2 v[192:193], v170, v[174:175], s[22:23] offset:0 sc0\n"
    "s_or_b64 exec, s[50:51], 1\n"
    "global_atomic_add_x2 v[194:195], v170, v[174:175], s[22:23] offset:1024 sc0\n"
    "s_or_b64 exec, s[52:53], 1\n"
    "global_atomic_add_x2 v[196:197], v170, v[174:175], s[22:23] offset:2048 sc0\n"
    "s_or_b64 exec, s[54:55], 1\n"
    "global_atomic_add_x2 v[198:199], v170, v[174:175], s[22:23] offset:3072 sc0\n"
    "s_sleep 3\n"
    ".Lfd_rpoll:\n"
    "s_or_b64 exec, s[48:49], 1\n"
    "global_atomic_add_x2 v[200:201], v170, v[174:175], s[22:23] offset:0 sc0\n"
    "s_or_b64 exec, s[50:51], 1\n"
    "global_atomic_add_x2 v[202:203], v170, v[174:175], s[22:23] offset:1024 sc0\n"
    "s_or_b64 exec, s[52:53], 1\n"
    "global_atomic_add_x2 v[204:205], v170, v[174:175], s[22:23] offset:2048 sc0\n"
    "s_or_b64 exec, s[54:55], 1\n"
    "global_atomic_add_x2 v[206:207], v170, v[174:175], s[22:23] offset:3072 sc0\n"
    "s_waitcnt vmcnt(8)\n"
    "s_mov_b64 exec, s[48:49]\n"
    "v_xor_b32 v176, v172, v184\n"
    "v_xor_b32 v177, v173, v185\n"
    "v_bitop3_b32 v184, v176, v177, s45 bitop3:0xa8\n"
    "v_cmp_ne_u32 vcc, 0, v184\n"
    "s_wqm_b64 s[48:49], vcc\n"
    "s_mov_b64 exec, s[50:51]\n"
    "v_xor_b32 v178, v172, v186\n"
    "v_xor_b32 v179, v173, v187\n"
    "v_bitop3_b32 v186, v178, v179, s45 bitop3:0xa8\n"
    "v_cmp_ne_u32 vcc, 0, v186\n"
    "s_wqm_b64 s[50:51], vcc\n"
    "s_mov_b64 exec, s[52:53]\n"
    "v_xor_b32 v180, v172, v188\n"
    "v_xor_b32 v181, v173, v189\n"
    "v_bitop3_b32 v188, v180, v181, s45 bitop3:0xa8\n"
    "v_cmp_ne_u32 vcc, 0, v188\n"
    "s_wqm_b64 s[52:53], vcc\n"
    "s_mov_b64 exec, s[54:55]\n"
    "v_xor_b32 v182, v172, v190\n"
    "v_xor_b32 v183, v173, v191\n"
    "v_bitop3_b32 v190, v182, v183, s45 bitop3:0xa8\n"
    "v_cmp_ne_u32 vcc, 0, v190\n"
    "s_wqm_b64 s[54:55], vcc\n"
    "s_or_b64 vcc, s[48:49], s[50:51]\n"
    "s_or_b64 s[56:57], s[52:53], s[54:55]\n"
    "s_or_b64 vcc, vcc, s[56:57]\n"
    "s_cbranch_scc0 .Lfd_rok\n"
    "s_or_b64 exec, s[48:49], 1\n"
    "global_atomic_add_x2 v[184:185], v170, v[174:175], s[22:23] offset:0 sc0\n"
    "s_or_b64 exec, s[50:51], 1\n"
    "global_atomic_add_x2 v[186:187], v170, v[174:175], s[22:23] offset:1024 sc0\n"
    "s_or_b64 exec, s[52:53], 1\n"
    "global_atomic_add_x2 v[188:189], v170, v[174:175], s[22:23] offset:2048 sc0\n"
    "s_or_b64 exec, s[54:55], 1\n"
    "global_atomic_add_x2 v[190:191], v170, v[174:175], s[22:23] offset:3072 sc0\n"
    "s_waitcnt vmcnt(8)\n"
    "s_mov_b64 exec, s[48:49]\n"
    "v_xor_b32 v176, v172, v192\n"
    "v_xor_b32 v177, v173, v193\n"
    "v_bitop3_b32 v192, v176, v177, s45 bitop3:0xa8\n"
    "v_cmp_ne_u32 vcc, 0, v192\n"
    "s_wqm_b64 s[48:49], vcc\n"
    "s_mov_b64 exec, s[50:51]\n"
    "v_xor_b32 v178, v172, v194\n"
    "v_xor_b32 v179, v173, v195\n"
    "v_bitop3_b32 v194, v178, v179, s45 bitop3:0xa8\n"
    "v_cmp_ne_u32 vcc, 0, v194\n"
    "s_wqm_b64 s[50:51], vcc\n"
    "s_mov_b64 exec, s[52:53]\n"
    "v_xor_b32 v180, v172, v196\n"
    "v_xor_b32 v181, v173, v197\n"
    "v_bitop3_b32 v196, v180, v181, s45 bitop3:0xa8\n"
    "v_cmp_ne_u32 vcc, 0, v196\n"
    "s_wqm_b64 s[52:53], vcc\n"
    "s_mov_b64 exec, s[54:55]\n"
    "v_xor_b32 v182, v172, v198\n"
    "v_xor_b32 v183, v173, v199\n"
    "v_bitop3_b32 v198, v182, v183, s45 bitop3:0xa8\n"
    "v_cmp_ne_u32 vcc, 0, v198\n"
    "s_wqm_b64 s[54:55], vcc\n"
    "s_or_b64 vcc, s[48:49], s[50:51]\n"
    "s_or_b64 s[56:57], s[52:53], s[54:55]\n"
    "s_or_b64 vcc, vcc, s[56:57]\n"
    "s_cbranch_scc0 .Lfd_rok\n"
    "s_or_b64 exec, s[48:49], 1\n"
    "global_atomic_add_x2 v[192:193], v170, v[174:175], s[22:23] offset:0 sc0\n"
    "s_or_b64 exec, s[50:51], 1\n"
    "global_atomic_add_x2 v[194:195], v170, v[174:175], s[22:23] offset:1024 sc0\n"
    "s_or_b64 exec, s[52:53], 1\n"
    "global_atomic_add_x2 v[196:197], v170, v[174:175], s[22:23] offset:2048 sc0\n"
    "s_or_b64 exec, s[54:55], 1\n"
    "global_atomic_add_x2 v[198:199], v170, v[174:175], s[22:23] offset:3072 sc0\n"
    "s_waitcnt vmcnt(8)\n"
    "s_mov_b64 exec, s[48:49]\n"
    "v_xor_b32 v176, v172, v200\n"
    "v_xor_b32 v177, v173, v201\n"
    "v_bitop3_b32 v200, v176, v177, s45 bitop3:0xa8\n"
    "v_cmp_ne_u32 vcc, 0, v200\n"
    "s_wqm_b64 s[48:49], vcc\n"
    "s_mov_b64 exec, s[50:51]\n"
    "v_xor_b32 v178, v172, v202\n"
    "v_xor_b32 v179, v173, v203\n"
    "v_bitop3_b32 v202, v178, v179, s45 bitop3:0xa8\n"
    "v_cmp_ne_u32 vcc, 0, v202\n"
    "s_wqm_b64 s[50:51], vcc\n"
    "s_mov_b64 exec, s[52:53]\n"
    "v_xor_b32 v180, v172, v204\n"
    "v_xor_b32 v181, v173, v205\n"
    "v_bitop3_b32 v204, v180, v181, s45 bitop3:0xa8\n"
    "v_cmp_ne_u32 vcc, 0, v204\n"
    "s_wqm_b64 s[52:53], vcc\n"
    "s_mov_b64 exec, s[54:55]\n"
    "v_xor_b32 v182, v172, v206\n"
    "v_xor_b32 v183, v173, v207\n"
    "v_bitop3_b32 v206, v182, v183, s45 bitop3:0xa8\n"
    "v_cmp_ne_u32 vcc, 0, v206\n"
    "s_wqm_b64 s[54:55], vcc\n"
    "s_or_b64 vcc, s[48:49], s[50:51]\n"
    "s_or_b64 s[56:57], s[52:53], s[54:55]\n"
    "s_or_b64 vcc, vcc, s[56:57]\n"
    "s_cbranch_scc0 .Lfd_rok\n"
    "s_sub_u32 s44, s44, 1\n"
    "s_cbranch_scc0 .Lfd_rpoll\n"
    ".Lfd_rok:\n"
    "s_mov_b64 exec, -1\n"
    "v_pk_add_f32 v[176:177], v[176:177], v[178:179]\n"
    "v_pk_add_f32 v[180:181], v[180:181], v[182:183]\n"
    "s_mov_b32 s46, 0xff\n"
    "s_mov_b32 s47, 0xff\n"
    "v_pk_add_f32 v[176:177], v[176:177], v[180:181]\n"
    "s_nop 1\n"
    "v_permlane32_swap_b32 v176, v177\n"
    "v_add_f32 v176, v176, v177\n"
    "s_nop 1\n"
    "v_add_f32_dpp v177, v176, v176 row_ror:8 row_mask:0xf bank_mask:0xf\n"
    "v_add_f32_dpp v176, v176, v176 row_ror:8 row_mask:0xf bank_mask:0xf\n"
    "s_nop 1\n"
    "v_permlane16_swap_b32 v176, v177\n"
    "v_add_f32 v176, v176, v177\n"
    "s_mov_b64 exec, s[46:47]\n"
    "ds_write_b32 v171, v176 offset:2048\n"
    "s_cmp_eq_u32 s61, 0\n"
    "s_waitcnt lgkmcnt(0)\n"
    "s_barrier\n"
    "s_cbranch_scc0 .Lfd_bump\n"
    "s_mov_b64 exec, 0xffff\n"
    "ds_read_b128 v[16:19], v3 offset:2048\n"
    "s_waitcnt lgkmcnt(0)\n"
    "v_add_f32 v16, v16, v17\n"
    "v_add_f32 v18, v18, v19\n"
    "v_cvt_pk_bf16_f32 v17, v18, v16\n"
    "v_lshlrev_b32 v16, 16, v17\n"
    "v_and_b32 v17, 0xffff0000, v17\n"
    "v_mul_f32 v18, 0xbfb8aa3b, v16\n"
    "v_exp_f32 v18, v18\n"
    "s_nop 0\n"
    "v_add_f32 v18, 1.0, v18\n"
    "v_rcp_f32 v18, v18\n"
    "s_nop 0\n"
    "v_mul_f32 v16, v16, v18\n"
    "v_cvt_pk_bf16_f32 v16, v16, v16\n"
    "v_lshlrev_b32 v16, 16, v16\n"
    "v_mul_f32 v16, v16, v17\n"
    "v_cvt_pk_bf16_f32 v16, v16, v16\n"
    "v_lshlrev_b32 v16, 16, v16\n"
    "v_add_f32 v16, v16, v43\n"
    "v_cvt_pk_bf16_f32 v16, v16, v16\n"
    "global_store_short v42, v16, s[20:21] sc1\n"
    "s_endpgm\n"
    ".Lfd_bump:\n"
    "s_lshl2_add_u32 s39, s39, s61\n"
    "s_cmp_eq_u32 s39, 1\n"
    "s_cbranch_scc0 .Lfd_end\n"
    "s_mov_b64 exec, 1\n"
    "v_mov_b64 v[16:17], s[24:25]\n"
    "v_mov_b64 v[18:19], s[26:27]\n"
    "global_store_dwordx4 v3, v[16:19], s[22:23] sc1\n"
    ".Lfd_end:\n"
    "s_endpgm\n"
    ".Lfd_codeend:\n"
    : "+{s[0:1]}"(ka), "+{s2}"(bid), "+{v0}"(tid)
    : "s"((unsigned)(size_t)(__attribute__((address_space(3))) char*)lds)
    : "v1", "v2", "v3", "v4", "v5", "v6", "v7", "v8", "v9", "v10", "v11", "v12", "v13", "v14", "v15", "v16", "v17", "v18", "v19", "v20", "v21", "v22", "v23", "v24",
      "v25", "v26", "v27", "v28", "v29", "v30", "v31", "v32", "v33", "v34", "v35", "v36", "v37", "v38", "v39", "v40", "v41", "v42", "v43", "v44", "v45", "v46", "v47", "v48",
      "v49", "v50", "v51", "v52", "v53", "v54", "v55", "v56", "v57", "v58", "v59", "v60", "v61", "v62", "v63", "v64", "v65", "v66", "v67", "v68", "v69", "v70", "v71", "v72",
      "v73", "v74", "v75", "v76", "v77", "v78", "v79", "v80", "v81", "v82", "v83", "v84", "v85", "v86", "v87", "v88", "v89", "v90", "v91", "v92", "v93", "v94", "v95", "v96",
      "v97", "v98", "v99", "v100", "v101", "v102", "v103", "v104", "v105", "v106", "v107", "v108", "v109", "v110", "v111", "v112", "v113", "v114", "v115", "v116", "v117", "v118", "v119", "v120",
      "v121", "v122", "v123", "v124", "v125", "v126", "v127", "v128", "v129", "v130", "v131", "v132", "v133", "v134", "v135", "v136", "v137", "v138", "v139", "v140", "v141", "v142", "v143", "v144",
      "v145", "v146", "v147", "v148", "v149", "v150", "v151", "v152", "v153", "v154", "v155", "v156", "v157", "v158", "v159", "v160", "v161", "v162", "v163", "v164", "v165", "v166", "v167", "v168",
      "v169", "v170", "v171", "v172", "v173", "v174", "v175", "v176", "v177", "v178", "v179", "v180", "v181", "v182", "v183", "v184", "v185", "v186", "v187", "v188", "v189", "v190", "v191", "v192",
      "v193", "v194", "v195", "v196", "v197", "v198", "v199", "v200", "v201", "v202", "v203", "v204", "v205", "v206", "v207", "v208", "v209", "v210", "v211", "v212", "v213", "v214", "v215", "a0",
      "a1", "a2", "a3", "a4", "a5", "a6", "a7", "a8", "a9", "a10", "a11", "a12", "a13", "a14", "a15", "a16", "a17", "a18", "a19", "a20", "a21", "a22", "a23", "a24",
      "a25", "a26", "a27", "a28", "a29", "a30", "a31", "a32", "a33", "a34", "a35", "a36", "a37", "a38", "a39", "a40", "a41", "a42", "a43", "a44", "a45", "a46", "a47", "a48",
      "a49", "a50", "a51", "a52", "a53", "a54", "a55", "a56", "a57", "a58", "a59", "a60", "a61", "a62", "a63", "a64", "a65", "a66", "a67", "a68", "a69", "a70", "a71", "a72",
      "a73", "a74", "a75", "a76", "a77", "a78", "a79", "a80", "a81", "a82", "a83", "a84", "a85", "a86", "a87", "a88", "a89", "a90", "a91", "a92", "a93", "a94", "a95", "a96",
      "a97", "a98", "a99", "a100", "a101", "a102", "a103", "a104", "a105", "a106", "a107", "a108", "a109", "a110", "a111", "a112", "a113", "a114", "a115", "a116", "a117", "a118", "a119", "a120",
      "a121", "a122", "a123", "a124", "a125", "a126", "a127", "a128", "a129", "a130", "a131", "a132", "a133", "a134", "a135", "a136", "a137", "a138", "a139", "a140", "a141", "a142", "a143", "a144",
      "a145", "a146", "a147", "a148", "a149", "a150", "a151", "a152", "a153", "a154", "a155", "a156", "a157", "a158", "a159", "a160", "a161", "a162", "a163", "a164", "a165", "a166", "a167", "a168",
      "a169", "a170", "a171", "a172", "a173", "a174", "a175", "a176", "a177", "a178", "a179", "a180", "a181", "a182", "a183", "a184", "a185", "a186", "a187", "a188", "a189", "a190", "a191", "a192",
      "a193", "a194", "a195", "a196", "a197", "a198", "a199", "a200", "a201", "a202", "a203", "a204", "a205", "a206", "a207", "a208", "a209", "a210", "a211", "a212", "a213", "a214", "a215", "a216",
      "a217", "a218", "a219", "a220", "a221", "a222", "a223", "a224", "a225", "a226", "a227", "a228", "a229", "a230", "a231", "a232", "a233", "a234", "a235", "a236", "a237", "a238", "a239", "a240",
      "a241", "a242", "a243", "a244", "a245", "a246", "a247", "a248", "a249", "a250", "a251", "a252", "a253", "a254", "a255", "s3", "s4", "s5", "s6", "s7", "s8", "s9", "s10", "s11",
      "s12", "s13", "s14", "s15", "s16", "s17", "s18", "s19", "s20", "s21", "s22", "s23", "s24", "s25", "s26", "s27", "s28", "s29", "s30", "s31", "s34", "s35", "s36", "s37",
      "s38", "s39", "s40", "s41", "s42", "s43", "s44", "s45", "s46", "s47", "s48", "s49", "s50", "s51", "s52", "s53", "s54", "s55", "s56", "s57", "s58", "s59", "s60", "s61",
      "s62", "s63", "s64", "s65", "s66", "s67", "s68", "s69", "s70", "s71", "s72", "s73", "s74", "s75", "s76", "s77", "s78", "s79", "s80", "s81", "s82", "s83", "s84", "s85",
      "s86", "s87", "s88", "s89", "s90", "s91", "s92", "s93", "s94", "s95", "s96", "s97", "s98", "s99", "vcc", "scc", "memory");
}

extern "C" void kernel_launch(void* const* d_in, const int* in_sizes, int n_in, void* d_out, int out_size, void* d_ws, size_t ws_size,
                              hipStream_t stream) {
  fused_decode<<<256, 256, 0, stream>>>(d_in[0], d_in[1], d_in[2], d_in[3], d_in[4], d_in[6], d_in[7], d_in[8], d_out, d_ws);
}
```
